```python
import torch

def _softmax(x, axis=-1):
    return torch.softmax(x, dim=axis)
def _gelu(x):
    return torch.nn.functional.gelu(x, approximate="tanh")
def _silu(x):
    return torch.nn.functional.silu(x)
def _sigmoid(x):
    return torch.sigmoid(x)
def _layernorm(x, axis=-1, eps=1e-5):
    m = x.mean(dim=axis, keepdim=True)
    v = x.var(dim=axis, keepdim=True, unbiased=False)
    return (x - m) / torch.sqrt(v + eps)
def _scatter_add(tgt, idx, src, axis=0):
    return tgt.clone().index_add_(axis, idx, src)
def _segment_sum(src, seg, n):
    out = torch.zeros((n,) + src.shape[1:], dtype=src.dtype)
    return out.index_add_(0, seg, src)
def _pad(x, pw):
    flat = tuple(v for pair in reversed(pw) for v in pair)
    return torch.nn.functional.pad(x, flat)

kernel_name = 'gated_mix__compressed_kv_cla__moe_arch_mod__prompt'

def setup_inputs():

    _w = (lambda a: (lambda t: t.to(torch.bfloat16) if t.is_floating_point() else t)(torch.as_tensor(a)))
    def _A_setup():
        B, H, Sq, Sk, D, M = 1, 32, 2048, 2048, 128, 512
        Dc = 4 * D
        return {
            "x": _w((lambda *s: torch.randn(*s, dtype=torch.bfloat16) * 0.05)(B, Sq, M)),
            "c_kv": _w((lambda *s: torch.randn(*s, dtype=torch.bfloat16) * 0.8)(B, Sk, Dc)),
            "w_q": _w((lambda *s: torch.randn(*s, dtype=torch.bfloat16) * 6.4)(M, H, D) * (M ** -0.5)),
            "w_uk": _w((lambda *s: torch.randn(*s, dtype=torch.bfloat16) * 6.4)(Dc, H, D) * (Dc ** -0.5)),
            "w_uv": _w((lambda *s: torch.randn(*s, dtype=torch.bfloat16) * 0.8)(Dc, H, D) * (Dc ** -0.5)),
            "w_o": _w((lambda *s: torch.randn(*s, dtype=torch.bfloat16) * 0.8)(H, D, M) * ((H * D) ** -0.5)),
        }
    _A_in = _A_setup()

    def _B_setup():
        B, S, D, F, C = 1, 2048, 512, 1792, 2
        return {
            "x": _w((lambda *s: torch.randn(*s, dtype=torch.bfloat16) * 0.05)(B, S, D)),
            "w_r": _w((lambda *s: torch.randn(*s, dtype=torch.bfloat16) * 3.2)(D)),
            "w_gate": _w((lambda *s: torch.randn(*s, dtype=torch.bfloat16) * 0.2)(D, F)),
            "w_up": _w((lambda *s: torch.randn(*s, dtype=torch.bfloat16) * 0.2)(D, F)),
            "w_down": _w((lambda *s: torch.randn(*s, dtype=torch.bfloat16) * 0.2)(F, D)),
        }
    _B_in = _B_setup()

    return {
        "A_x": _A_in["x"],
        "A_c_kv": _A_in["c_kv"],
        "A_w_q": _A_in["w_q"],
        "A_w_uk": _A_in["w_uk"],
        "A_w_uv": _A_in["w_uv"],
        "A_w_o": _A_in["w_o"],
        "B_x": _B_in["x"],
        "B_w_r": _B_in["w_r"],
        "B_w_gate": _B_in["w_gate"],
        "B_w_up": _B_in["w_up"],
        "B_w_down": _B_in["w_down"],
        "gate_w": _w((lambda *s: torch.randn(*s, dtype=torch.bfloat16) * 0.05)(512) * 16.0),
    }

def reference(A_x, A_c_kv, A_w_q, A_w_uk, A_w_uv, A_w_o, B_x, B_w_r, B_w_gate, B_w_up, B_w_down, gate_w):
    def _A_ref(x, c_kv, w_q, w_uk, w_uv, w_o):
        B, Sq, M = x.shape[0], x.shape[1], x.shape[2]
        Sk = c_kv.shape[1]
        H, D = w_q.shape[1], w_q.shape[2]
        q = torch.einsum("bqm,mhd->bhqd", x, w_q)
        k = torch.einsum("bkc,chd->bhkd", c_kv, w_uk)
        v = torch.einsum("bkc,chd->bhkd", c_kv, w_uv)
        scores = torch.einsum("bhqd,bhkd->bhqk", q, k) * (D ** -0.5)
        off = Sk - Sq
        qi = torch.arange(Sq)[:, None]
        kj = torch.arange(Sk)[None, :]
        cmask = kj <= (qi + off)
        scores = torch.where(cmask[None, None], scores, -1e9)
        p = _softmax(scores, axis=-1)
        ctx = torch.einsum("bhqk,bhkd->bhqd", p, v)
        return torch.einsum("bhqd,hdm->bqm", ctx, w_o)

    def _B_ref(x, w_r, w_gate, w_up, w_down):
        B, S, D = x.shape
        C = 2
        r = x @ w_r
        idx = torch.argsort(-r, axis=-1)[:, :C]
        idx_e = (lambda x, n, axis: torch.repeat_interleave(x, n, dim=axis))(idx.reshape(B, C, 1), D, axis=2)
        x_sel = (lambda x, i, axis: torch.take_along_dim(x, i, dim=axis))(x, idx_e, axis=1)
        g = _silu(x_sel @ w_gate)
        u = x_sel @ w_up
        y = (g * u) @ w_down
        r_sel = _sigmoid((lambda x, i, axis: torch.take_along_dim(x, i, dim=axis))(r, idx, axis=1))
        y = y * r_sel.reshape(B, C, 1)
        b_off = (torch.arange(B) * S).reshape(B, 1)
        flat_idx = (idx + b_off).reshape(B * C)
        zero = (x * 0.0).reshape(B * S, D)
        upd = _scatter_add(zero, flat_idx, y.reshape(B * C, D), axis=0)
        return x + upd.reshape(B, S, D)

    x = A_x
    g = _sigmoid((x * gate_w).sum(-1, keepdims=True))
    a = _A_ref(x, A_c_kv, A_w_q, A_w_uk, A_w_uv, A_w_o)
    b = _B_ref(x, B_w_r, B_w_gate, B_w_up, B_w_down)
    return g * a + (1 - g) * b
```

```cpp
#include <hip/hip_runtime.h>
#include <hip/hip_bf16.h>
#include <stdint.h>

typedef short bf16x8 __attribute__((ext_vector_type(8)));
typedef float f32x16 __attribute__((ext_vector_type(16)));
typedef float f32x4 __attribute__((ext_vector_type(4)));
typedef unsigned u32x4 __attribute__((ext_vector_type(4)));
typedef unsigned short u16;
typedef __bf16 bf16x2 __attribute__((ext_vector_type(2)));
typedef float f32x2 __attribute__((ext_vector_type(2)));

#define DI __device__ __forceinline__
constexpr int S = 2048, H = 32, D = 128, M = 512, HD = H * D, F = 1792;

DI float bf2f(u16 b) { return __uint_as_float((unsigned)b << 16); }
DI unsigned cvtpk(float lo, float hi) { f32x2 v = {lo, hi}; bf16x2 b = __builtin_convertvector(v, bf16x2); return __builtin_bit_cast(unsigned, b); }
DI u16 f2bf(float f) { return (u16)(cvtpk(f, 0.f) & 0xffffu); }
DI float rbf(float f) { return bf2f(f2bf(f)); }
#define MFMA32(a, b, c) __builtin_amdgcn_mfma_f32_32x32x16_bf16(a, b, c, 0, 0, 0)

constexpr size_t WS_Q = 0;
constexpr size_t WS_K = WS_Q + (size_t)S * HD * 2;
constexpr size_t WS_V = WS_K + (size_t)S * HD * 2;
constexpr size_t WS_CTX = WS_V + (size_t)S * HD * 2;
constexpr size_t WS_R = WS_CTX + (size_t)S * HD * 2;
constexpr size_t WS_G = WS_R + S * 4;
constexpr size_t WS_IDX = WS_G + S * 4;
constexpr size_t WS_HB = WS_IDX + 64;
constexpr size_t WS_Y = WS_HB + 2 * F * 4;

constexpr float QSCALE = 0.08838834764831845f * 1.4426950408889634f;

__global__ __launch_bounds__(256) void proj_naive(const u16* __restrict__ x, const u16* __restrict__ ckv, const u16* __restrict__ wq, const u16* __restrict__ wk,
                                                  const u16* __restrict__ wv, u16* __restrict__ Qf, u16* __restrict__ Kf, u16* __restrict__ Vf) {
    const int lane = threadIdx.x & 63, wid = threadIdx.x >> 6;
    const int tile = blockIdx.x * 4 + wid;
    const int which = tile / (64 * 128), rem = tile % (64 * 128), mb = rem / 128, nb = rem % 128;
    const u16* act = which == 0 ? x : ckv;
    const u16* W = which == 0 ? wq : (which == 1 ? wk : wv);
    const int r = lane & 31, hi = lane >> 5;
    int ncol;
    if (which < 2) { const int a = r >> 3, h2 = (r >> 2) & 1, b = r & 3; ncol = 16 * (a >> 1) + 8 * h2 + 4 * (a & 1) + b; } else ncol = r;
    f32x16 acc = {};
    for (int k0 = 0; k0 < M; k0 += 16) {
        bf16x8 af = *(const bf16x8*)(act + (size_t)(mb * 32 + r) * M + k0 + 8 * hi);
        bf16x8 wf;
#pragma unroll
        for (int j = 0; j < 8; ++j) wf[j] = (short)W[(size_t)(k0 + 8 * hi + j) * HD + nb * 32 + ncol];
        if (which < 2) acc = MFMA32(wf, af, acc); else acc = MFMA32(af, wf, acc);
    }
    const int h = nb >> 2, db = nb & 3;
    if (which < 2) {
        const float sc = which == 0 ? QSCALE : 1.f;
        u16* base = which == 0 ? Qf + ((size_t)(h * 64 + mb) * 8) * 512 : Kf + ((size_t)(h * 64 + mb) * 8) * 512;
#pragma unroll
        for (int s = 0; s < 2; ++s) {
            u32x4 w;
#pragma unroll
            for (int e = 0; e < 4; ++e) w[e] = cvtpk(acc[8 * s + 2 * e] * sc, acc[8 * s + 2 * e + 1] * sc);
            *(u32x4*)(base + (size_t)(2 * db + s) * 512 + lane * 8) = w;
        }
    } else {
        const int t64 = mb >> 1, kb = mb & 1;
#pragma unroll
        for (int s = 0; s < 2; ++s) {
            u32x4 w;
#pragma unroll
            for (int e = 0; e < 4; ++e) w[e] = cvtpk(acc[8 * s + 2 * e], acc[8 * s + 2 * e + 1]);
            *(u32x4*)(Vf + ((((size_t)(h * 32 + t64) * 4 + 2 * kb + s) * 4 + db) * 64 + lane) * 8) = w;
        }
    }
}

__global__ __launch_bounds__(256) void attn_naive(const u16* __restrict__ Qf, const u16* __restrict__ Kf, const u16* __restrict__ Vf, u16* __restrict__ ctx) {
    const int lane = threadIdx.x & 63, wid = threadIdx.x >> 6;
    const int item = blockIdx.x * 4 + wid;
    const int h = item & 31, qb = 63 - (item >> 5);
    const int r = lane & 31, hi = lane >> 5;
    bf16x8 qf[8];
#pragma unroll
    for (int d0 = 0; d0 < 8; ++d0) qf[d0] = *(const bf16x8*)(Qf + ((size_t)(h * 64 + qb) * 8 + d0) * 512 + lane * 8);
    f32x16 o[4] = {};
    float m = -1e30f, l = 0.f;
    const int qrow = qb * 32 + r;
    for (int kb = 0; kb <= qb; ++kb) {
        f32x16 s = {};
#pragma unroll
        for (int d0 = 0; d0 < 8; ++d0) {
            bf16x8 kf = *(const bf16x8*)(Kf + ((size_t)(h * 64 + kb) * 8 + d0) * 512 + lane * 8);
            s = MFMA32(kf, qf[d0], s);
        }
        if (kb == qb) {
#pragma unroll
            for (int i = 0; i < 16; ++i) { const int key = kb * 32 + (i & 3) + 8 * (i >> 2) + 4 * hi; if (key > qrow) s[i] = -INFINITY; }
        }
        float pm = s[0];
#pragma unroll
        for (int i = 1; i < 16; ++i) pm = fmaxf(pm, s[i]);
        pm = fmaxf(pm, __shfl_xor(pm, 32));
        const float mn = fmaxf(m, pm), alpha = exp2f(m - mn);
        m = mn;
        float ps = 0.f;
#pragma unroll
        for (int i = 0; i < 16; ++i) { s[i] = exp2f(s[i] - mn); ps += s[i]; }
        ps += __shfl_xor(ps, 32);
        l = l * alpha + ps;
#pragma unroll
        for (int db = 0; db < 4; ++db)
#pragma unroll
            for (int i = 0; i < 16; ++i) o[db][i] *= alpha;
        const int t64 = kb >> 1, kbb = kb & 1;
#pragma unroll
        for (int st = 0; st < 2; ++st) {
            u32x4 w;
#pragma unroll
            for (int e = 0; e < 4; ++e) w[e] = cvtpk(s[8 * st + 2 * e], s[8 * st + 2 * e + 1]);
            bf16x8 pf = __builtin_bit_cast(bf16x8, w);
#pragma unroll
            for (int db = 0; db < 4; ++db) {
                bf16x8 vf = *(const bf16x8*)(Vf + ((((size_t)(h * 32 + t64) * 4 + 2 * kbb + st) * 4 + db) * 64 + lane) * 8);
                o[db] = MFMA32(vf, pf, o[db]);
            }
        }
    }
    const float il = 1.f / l;
#pragma unroll
    for (int db = 0; db < 4; ++db)
#pragma unroll
        for (int g = 0; g < 4; ++g) {
            uint2 w; w.x = cvtpk(o[db][4 * g] * il, o[db][4 * g + 1] * il); w.y = cvtpk(o[db][4 * g + 2] * il, o[db][4 * g + 3] * il);
            *(uint2*)(ctx + (size_t)qrow * HD + h * 128 + db * 32 + 8 * g + 4 * hi) = w;
        }
}

__global__ __launch_bounds__(256) void router_gate(const u16* __restrict__ xa, const u16* __restrict__ xb, const u16* __restrict__ wr, const u16* __restrict__ gw,
                                                   float* __restrict__ rout, float* __restrict__ gout) {
    const int lane = threadIdx.x & 63, row = blockIdx.x * 4 + (threadIdx.x >> 6);
    bf16x8 a = *(const bf16x8*)(xa + (size_t)row * M + lane * 8), b = *(const bf16x8*)(xb + (size_t)row * M + lane * 8);
    bf16x8 w = *(const bf16x8*)(wr + lane * 8), g = *(const bf16x8*)(gw + lane * 8);
    float sr = 0.f, sg = 0.f;
#pragma unroll
    for (int j = 0; j < 8; ++j) { sr += bf2f((u16)b[j]) * bf2f((u16)w[j]); sg += rbf(bf2f((u16)a[j]) * bf2f((u16)g[j])); }
#pragma unroll
    for (int o = 32; o; o >>= 1) { sr += __shfl_xor(sr, o); sg += __shfl_xor(sg, o); }
    if (lane == 0) { rout[row] = rbf(sr); const float z = rbf(sg); gout[row] = rbf(1.f / (1.f + __expf(-z))); }
}

__global__ __launch_bounds__(256) void ffn1(const float* __restrict__ r, const u16* __restrict__ xb, const u16* __restrict__ wg, const u16* __restrict__ wu,
                                            int* __restrict__ idx_out, float* __restrict__ hbuf) {
    __shared__ float sv[256]; __shared__ int si[256]; __shared__ int top[2]; __shared__ float red[4][4][64];
    const int t = threadIdx.x;
    int excl = -1;
    for (int pass = 0; pass < 2; ++pass) {
        float bv = -INFINITY; int bi = 0x7fffffff;
        for (int i = t; i < S; i += 256) { const float v = r[i]; if (i != excl && (v > bv || (v == bv && i < bi))) { bv = v; bi = i; } }
        sv[t] = bv; si[t] = bi; __syncthreads();
        for (int o = 128; o; o >>= 1) { if (t < o) { if (sv[t + o] > sv[t] || (sv[t + o] == sv[t] && si[t + o] < si[t])) { sv[t] = sv[t + o]; si[t] = si[t + o]; } } __syncthreads(); }
        if (t == 0) top[pass] = si[0];
        __syncthreads();
        excl = top[0];
    }
    const int i0 = top[0], i1 = top[1];
    if (blockIdx.x == 0 && t == 0) { idx_out[0] = i0; idx_out[1] = i1; }
    const int c = blockIdx.x * 64 + (t & 63), kq = t >> 6;
    float g0 = 0, g1 = 0, u0 = 0, u1 = 0;
    for (int k = kq * 128; k < kq * 128 + 128; ++k) {
        const float a0 = bf2f(xb[(size_t)i0 * M + k]), a1 = bf2f(xb[(size_t)i1 * M + k]);
        const float wgv = bf2f(wg[(size_t)k * F + c]), wuv = bf2f(wu[(size_t)k * F + c]);
        g0 += a0 * wgv; g1 += a1 * wgv; u0 += a0 * wuv; u1 += a1 * wuv;
    }
    red[kq][0][t & 63] = g0; red[kq][1][t & 63] = g1; red[kq][2][t & 63] = u0; red[kq][3][t & 63] = u1;
    __syncthreads();
    if (t < 128) {
        const int tok = t >> 6, cc = t & 63;
        const float g = rbf(red[0][tok][cc] + red[1][tok][cc] + red[2][tok][cc] + red[3][tok][cc]);
        const float u = rbf(red[0][2 + tok][cc] + red[1][2 + tok][cc] + red[2][2 + tok][cc] + red[3][2 + tok][cc]);
        const float sg = rbf(g / (1.f + __expf(-g)));
        hbuf[tok * F + blockIdx.x * 64 + cc] = rbf(sg * u);
    }
}

__global__ __launch_bounds__(256) void ffn2(const float* __restrict__ r, const int* __restrict__ idx, const float* __restrict__ hbuf, const u16* __restrict__ wd, float* __restrict__ y) {
    __shared__ float red[4][2][64];
    const int t = threadIdx.x, c = blockIdx.x * 64 + (t & 63), kq = t >> 6;
    float y0 = 0, y1 = 0;
    for (int k = kq * 448; k < kq * 448 + 448; ++k) { const float w = bf2f(wd[(size_t)k * M + c]); y0 += hbuf[k] * w; y1 += hbuf[F + k] * w; }
    red[kq][0][t & 63] = y0; red[kq][1][t & 63] = y1;
    __syncthreads();
    if (t < 128) {
        const int tok = t >> 6, cc = t & 63;
        const float v = rbf(red[0][tok][cc] + red[1][tok][cc] + red[2][tok][cc] + red[3][tok][cc]);
        const float rs = rbf(1.f / (1.f + __expf(-r[idx[tok]])));
        y[tok * M + blockIdx.x * 64 + cc] = rbf(v * rs);
    }
}

__global__ __launch_bounds__(256) void outproj_naive(const u16* __restrict__ ctx, const u16* __restrict__ wo, const u16* __restrict__ xb, const float* __restrict__ g,
                                                     const int* __restrict__ idx, const float* __restrict__ y, u16* __restrict__ out) {
    const int lane = threadIdx.x & 63, wid = threadIdx.x >> 6;
    const int tile = blockIdx.x * 4 + wid;
    const int mb = tile >> 4, nb = tile & 15;
    const int r = lane & 31, hi = lane >> 5;
    f32x16 acc = {};
    for (int k0 = 0; k0 < HD; k0 += 16) {
        bf16x8 af = *(const bf16x8*)(ctx + (size_t)(mb * 32 + r) * HD + k0 + 8 * hi);
        bf16x8 wf;
#pragma unroll
        for (int j = 0; j < 8; ++j) wf[j] = (short)wo[(size_t)(k0 + 8 * hi + j) * M + nb * 32 + r];
        acc = MFMA32(wf, af, acc);
    }
    const int row = mb * 32 + r;
    const float gg = g[row], omg = rbf(1.f - gg);
    const int i0 = idx[0], i1 = idx[1];
#pragma unroll
    for (int q = 0; q < 4; ++q) {
        const int c0 = nb * 32 + 8 * q + 4 * hi;
        float o[4];
#pragma unroll
        for (int e = 0; e < 4; ++e) {
            const float a = rbf(acc[4 * q + e]);
            float b = bf2f(xb[(size_t)row * M + c0 + e]);
            float upd = b * 0.f;
            if (row == i0) upd = rbf(upd + y[c0 + e]);
            if (row == i1) upd = rbf(upd + y[M + c0 + e]);
            b = rbf(b + upd);
            o[e] = rbf(gg * a) + rbf(omg * b);
        }
        uint2 w; w.x = cvtpk(o[0], o[1]); w.y = cvtpk(o[2], o[3]);
        *(uint2*)(out + (size_t)row * M + c0) = w;
    }
}

extern "C" void kernel_launch(void* const* d_in, const int* in_sizes, int n_in, void* d_out, int out_size, void* d_ws, size_t ws_size, hipStream_t stream) {
    const u16* x = (const u16*)d_in[0]; const u16* ckv = (const u16*)d_in[1]; const u16* wq = (const u16*)d_in[2]; const u16* wk = (const u16*)d_in[3];
    const u16* wv = (const u16*)d_in[4]; const u16* wo = (const u16*)d_in[5]; const u16* xb = (const u16*)d_in[0]; const u16* wr = (const u16*)d_in[7];
    const u16* wg = (const u16*)d_in[8]; const u16* wu = (const u16*)d_in[9]; const u16* wd = (const u16*)d_in[10]; const u16* gw = (const u16*)d_in[11];
    char* ws = (char*)d_ws;
    u16* Qf = (u16*)(ws + WS_Q); u16* Kf = (u16*)(ws + WS_K); u16* Vf = (u16*)(ws + WS_V); u16* ctx = (u16*)(ws + WS_CTX);
    float* r = (float*)(ws + WS_R); float* g = (float*)(ws + WS_G); int* idx = (int*)(ws + WS_IDX); float* hb = (float*)(ws + WS_HB); float* y = (float*)(ws + WS_Y);
    proj_naive<<<3 * 64 * 128 / 4, 256, 0, stream>>>(x, ckv, wq, wk, wv, Qf, Kf, Vf);
    router_gate<<<S / 4, 256, 0, stream>>>(x, xb, wr, gw, r, g);
    ffn1<<<F / 64, 256, 0, stream>>>(r, xb, wg, wu, idx, hb);
    ffn2<<<M / 64, 256, 0, stream>>>(r, idx, hb, wd, y);
    attn_naive<<<32 * 64 / 4, 256, 0, stream>>>(Qf, Kf, Vf, ctx);
    outproj_naive<<<64 * 16 / 4, 256, 0, stream>>>(ctx, wo, xb, g, idx, y, (u16*)d_out);
}
```

```cpp
#include <hip/hip_runtime.h>
#include <hip/hip_bf16.h>
#include <stdint.h>

typedef short bf16x8 __attribute__((ext_vector_type(8)));
typedef short s16x4 __attribute__((ext_vector_type(4)));
typedef short v4i16_t __attribute__((ext_vector_type(4)));
typedef float f32x4 __attribute__((ext_vector_type(4)));
typedef float f32x2 __attribute__((ext_vector_type(2)));
typedef unsigned u32x4 __attribute__((ext_vector_type(4)));
typedef unsigned u32x2 __attribute__((ext_vector_type(2)));
typedef unsigned short u16;
typedef __bf16 bf16x2 __attribute__((ext_vector_type(2)));
typedef __attribute__((address_space(3))) const bf16x8* lfrag_p;
typedef __attribute__((address_space(3))) char* lds_p;
typedef __attribute__((address_space(3))) f32x4* lf4_p;
typedef __attribute__((address_space(3))) f32x2* lf2_p;
typedef __attribute__((address_space(1))) unsigned gu32;
typedef __attribute__((address_space(1))) unsigned long long gu64;
typedef __amdgpu_buffer_rsrc_t rsrc_t;

#define DI __device__ __forceinline__
#define UNLIKELY(x) __builtin_expect(!!(x), 0)
constexpr int S = 2048, H = 32, D = 128, M = 512, HD = H * D, F = 1792;
constexpr float QSCALE = 0.08838834764831845f * 1.4426950408889634f;

DI float bf2f(u16 b) { return __uint_as_float((unsigned)b << 16); }
DI unsigned cvtpk(float lo, float hi) { f32x2 v = {lo, hi}; bf16x2 b = __builtin_convertvector(v, bf16x2); return __builtin_bit_cast(unsigned, b); }
DI float rbf(float f) { return __uint_as_float(cvtpk(f, 0.f) << 16); }
#define MFMA16(a, b, c) __builtin_amdgcn_mfma_f32_16x16x32_bf16(a, b, c, 0, 0, 0)
DI bf16x8 pack2(const f32x4& lo, const f32x4& hi) { u32x4 w = {cvtpk(lo[0], lo[1]), cvtpk(lo[2], lo[3]), cvtpk(hi[0], hi[1]), cvtpk(hi[2], hi[3])}; return __builtin_bit_cast(bf16x8, w); }
#define SBAR() __builtin_amdgcn_sched_barrier(0)
#define MFMA_ACC(acc, a, b) asm volatile("v_mfma_f32_16x16x32_bf16 %0, %1, %2, %0" : "+v"(acc) : "v"(a), "v"(b))
#define MFMA_INI(acc, a, b, c0) asm volatile("v_mfma_f32_16x16x32_bf16 %0, %1, %2, %3" : "=&v"(acc) : "v"(a), "v"(b), "v"(c0))
#define PIN(x) asm volatile("" : "+v"(x))
#define WAITV(n) asm volatile("s_waitcnt vmcnt(" #n ")" ::: "memory")
#define BARRIER() do { SBAR(); asm volatile("s_waitcnt lgkmcnt(0)\n\ts_barrier" ::: "memory"); SBAR(); } while (0)
#define MX3(a, b, c) __builtin_fmaxf(__builtin_fmaxf((a), (b)), (c))
template <int CTRL> DI float dpp_mov(float v) { return __uint_as_float(__builtin_amdgcn_update_dpp(0u, __float_as_uint(v), CTRL, 0xf, 0xf, false)); }
DI float wave_sum(float v) {
    v += dpp_mov<0xB1>(v); v += dpp_mov<0x4E>(v); v += dpp_mov<0x141>(v); v += dpp_mov<0x140>(v);
    { auto a = __builtin_amdgcn_permlane16_swap(__float_as_uint(v), __float_as_uint(v), false, false); v = __uint_as_float(a[0]) + __uint_as_float(a[1]); }
    { auto a = __builtin_amdgcn_permlane32_swap(__float_as_uint(v), __float_as_uint(v), false, false); v = __uint_as_float(a[0]) + __uint_as_float(a[1]); }
    return v;
}
DI rsrc_t mk_rsrc(const void* p, unsigned bytes) { return __builtin_amdgcn_make_buffer_rsrc((void*)p, (short)0, (int)bytes, 0x00020000); }
DI bf16x8 bld16(rsrc_t r, unsigned voff, unsigned soff) { return __builtin_bit_cast(bf16x8, __builtin_amdgcn_raw_buffer_load_b128(r, voff, soff, 0)); }
DI bf16x8 bld16c(rsrc_t r, unsigned voff, unsigned soff) { return __builtin_bit_cast(bf16x8, __builtin_amdgcn_raw_buffer_load_b128(r, voff, soff, 16)); }
DI void bst16(rsrc_t r, bf16x8 v, unsigned voff, unsigned soff) { __builtin_amdgcn_raw_buffer_store_b128(__builtin_bit_cast(u32x4, v), r, voff + soff, 0, 0); }
DI void bst16_wt(rsrc_t r, bf16x8 v, unsigned voff, unsigned soff) { __builtin_amdgcn_raw_buffer_store_b128(__builtin_bit_cast(u32x4, v), r, voff + soff, 0, 16); }
DI unsigned ld_agent(const void* p) { return __hip_atomic_load((gu32*)p, __ATOMIC_RELAXED, __HIP_MEMORY_SCOPE_AGENT); }
DI void set_flag(unsigned* fl, int idx, unsigned E) { __hip_atomic_store((gu32*)(fl + 32 * idx), E, __ATOMIC_RELAXED, __HIP_MEMORY_SCOPE_AGENT); }
DI void wait_flags(const unsigned* fl, int base, int stride, int n, unsigned E, int lane) {
    unsigned spins = 0;
    while (__ballot(lane < n && ld_agent(fl + 32 * (base + (lane < n ? lane : 0) * stride)) != E) != 0ull && ++spins < (1u << 22)) __builtin_amdgcn_s_sleep(1);
}
DI s16x4 trd(lds_p p) { return __builtin_bit_cast(s16x4, __builtin_amdgcn_ds_read_tr16_b64_v4i16((__attribute__((address_space(3))) v4i16_t*)p)); }
DI bf16x8 cat8(s16x4 a, s16x4 b) { return __builtin_shufflevector(a, b, 0, 1, 2, 3, 4, 5, 6, 7); }
#define SET_M0(base, imm) asm volatile("s_add_u32 m0, %0, %1" :: "s"(base), "i"(imm) : "scc")
#define SET_M0S(a, b) asm volatile("s_add_u32 m0, %0, %1" :: "s"(a), "s"(b) : "scc")
#define DMA_B(rs, voff, soff, OFF) asm volatile("buffer_load_dwordx4 %0, %1, %2 offen offset:" #OFF " lds" :: "v"(voff), "s"(rs), "s"(soff) : "memory")
#define DMA_BC(rs, voff, soff, OFF) asm volatile("buffer_load_dwordx4 %0, %1, %2 offen offset:" #OFF " sc1 lds" :: "v"(voff), "s"(rs), "s"(soff) : "memory")
#define DMA_P(rs, voff, soff, m0base, imm) asm volatile("s_add_u32 m0, %3, %4\n\ts_nop 0\n\tbuffer_load_dwordx4 %0, %1, %2 offen lds" :: "v"(voff), "s"(rs), "s"(soff), "s"(m0base), "i"(imm) : "memory", "scc")

#define STAMPL(c, i) do {} while (0)
#define STAMP(c, i) do {} while (0)
#define STAMPP(c, i) do {} while (0)
#define STAMPRT(c, i) do {} while (0)
#define STAMP_BEGIN(c) do {} while (0)
#define STAMP_END(c, dbg) do {} while (0)

constexpr size_t WS_Q = 0;
constexpr size_t WS_K = WS_Q + (size_t)S * HD * 2;
constexpr size_t WS_V = WS_K + (size_t)S * HD * 2;
constexpr size_t WS_CTX = WS_V + (size_t)S * HD * 2;
constexpr size_t WS_R = WS_CTX + (size_t)S * HD * 2;
constexpr size_t WS_G = WS_R + S * 8;
constexpr size_t WS_HB = WS_G + S * 8;
constexpr size_t WS_P2 = WS_HB + 28 * 8 * 4 * 64 * 4;
constexpr size_t WS_Y = WS_P2 + 28 * 2 * M * 4;
constexpr size_t WS_FLAG = 72u << 20;
constexpr int FL_KV = 1, FL_F1 = FL_KV + 256, FL_F2 = FL_F1 + 224, FL_CTX = FL_F2 + 224, FL_OUT = FL_CTX + 512;
constexpr size_t WS_SLAB = 80u << 20;

constexpr int PROJ_LDS = 163840;
struct ProjCtx {
    unsigned ts0, ts1; int lane;
    rsrc_t ra1, ra2, rwk, rwv, rwq;
    unsigned voffA[2], voffW;
    unsigned lds0, arow, arow2, wcol;
    int wid, rot;
};
template <int J> DI void proj_dma_a(const ProjCtx& c, const bool a2, const int t1, const unsigned aslot) {
    const rsrc_t ra = a2 ? c.ra2 : c.ra1;
    const unsigned so = (a2 ? c.arow2 : c.arow) + (c.wid * 4 + J) * 8192u + (unsigned)((t1 + c.rot) & 7) * 128u, m0 = c.lds0 + aslot + c.wid * 4096u;
    DMA_P(ra, c.voffA[J & 1], so, m0, J * 1024);
}
template <int J> DI void proj_dma_w(const ProjCtx& c, const rsrc_t rw, const int t1) {
    const unsigned so = c.wcol + ((unsigned)((t1 + c.rot) & 7) * 64u + (c.wid * 2 + (J & 1)) * 4u) * 8192u, m0 = c.lds0 + 98304u + (unsigned)(t1 & 1) * 32768u + c.wid * 2048u;
    DMA_P(rw, c.voffW, so, m0, (J >> 1) * 16384 + (J & 1) * 1024);
}
#define PROJ_ROT ((jj + 2 * (jj >> 3)) & 7)
constexpr int ATT_LDS = 131072;
constexpr float LSUM_LIMIT = 1.0995116e12f;
struct AttnCtx {
    unsigned ts0, ts1;
    bf16x8 qf[2][4], fr[8], ones;
    u32x4 pA[2], pB[2];
    f32x4 o[8][2], sA[2][2], sB[2][2], negm[2], l4[2], lR[2];
    float mref[2];
    unsigned long long bad, spmask;
    rsrc_t rc, rk, rv;
    unsigned kdst, vdst, voff;
    unsigned go1, go2, qlds;
    lds_p fbase;
    int wq, h, blkP, blkR, usw, delta, lane, rowblk;
    unsigned* f1flag; unsigned E;
};
DI float xmax(float v) {
    auto a = __builtin_amdgcn_permlane32_swap(__float_as_uint(v), __float_as_uint(v), false, false); v = __builtin_fmaxf(__uint_as_float(a[0]), __uint_as_float(a[1]));
    auto b = __builtin_amdgcn_permlane16_swap(__float_as_uint(v), __float_as_uint(v), false, false); return __builtin_fmaxf(__uint_as_float(b[0]), __uint_as_float(b[1]));
}
DI float rowmax8(const f32x4& a, const f32x4& b) { return xmax(__builtin_fmaxf(MX3(MX3(a[0], a[1], a[2]), a[3], b[0]), MX3(b[1], b[2], b[3]))); }
DI void attn_mask(f32x4 (&s)[2][2], int dq0, int lane) {
    const int b = dq0 + (lane & 15) - 4 * (lane >> 4);
#pragma unroll
    for (int kb = 0; kb < 2; ++kb)
#pragma unroll
        for (int qb = 0; qb < 2; ++qb)
#pragma unroll
            for (int e = 0; e < 4; ++e) s[kb][qb][e] = (16 * (kb - qb) + e > b) ? -INFINITY : s[kb][qb][e];
}
DI void attn_loadq_if(AttnCtx& c, int u1) {
    asm volatile("s_cmp_eq_u32 %8, %9\n\t"
                 "s_cbranch_scc1 .Lqp_rare%=\n"
                 ".Lqp_back%=:\n\t"
                 ".subsection 1\n"
                 ".Lqp_rare%=:\n\t"
                 "ds_read_b128 %0, %10\n\tds_read_b128 %1, %10 offset:1024\n\tds_read_b128 %2, %10 offset:2048\n\tds_read_b128 %3, %10 offset:3072\n\t"
                 "ds_read_b128 %4, %10 offset:4096\n\tds_read_b128 %5, %10 offset:5120\n\tds_read_b128 %6, %10 offset:6144\n\tds_read_b128 %7, %10 offset:7168\n\t"
                 "s_waitcnt lgkmcnt(0)\n\t"
                 "s_branch .Lqp_back%=\n\t"
                 ".subsection 0"
                 : "+v"(c.qf[0][0]), "+v"(c.qf[0][1]), "+v"(c.qf[0][2]), "+v"(c.qf[0][3]), "+v"(c.qf[1][0]), "+v"(c.qf[1][1]), "+v"(c.qf[1][2]), "+v"(c.qf[1][3])
                 : "s"(u1), "s"(c.usw), "v"(c.qlds) : "memory", "scc");
}
#define QF_OPS(c) "+v"((c).qf[0][0]), "+v"((c).qf[0][1]), "+v"((c).qf[0][2]), "+v"((c).qf[0][3]), "+v"((c).qf[1][0]), "+v"((c).qf[1][1]), "+v"((c).qf[1][2]), "+v"((c).qf[1][3])
DI void attn_store(rsrc_t rc, const f32x4 (&o)[8][2], const float (&il)[2], unsigned vb, int j0, int j1) {
#pragma unroll
    for (int qb = 0; qb < 2; ++qb)
#pragma unroll
        for (int j = 0; j < 4; ++j) if (j >= j0 && j < j1) bst16_wt(rc, pack2(o[2 * j][qb] * il[qb], o[2 * j + 1][qb] * il[qb]), vb, qb * 131072u + j * 1024u);
}
DI void attn_exact(const u16* __restrict__ xin, const u16* __restrict__ wq, rsrc_t rk, rsrc_t rv, rsrc_t rc, int h, int qb32, int lane) {
    const unsigned voff = lane * 16;
    bf16x8 qf[2][4];
    {
        const int lc = lane & 15, lg = lane >> 4;
        for (int ds = 0; ds < 4; ++ds) {
            f32x4 t[2][2] = {};
            for (int k0 = 0; k0 < M; k0 += 32) {
                bf16x8 wf[2];
#pragma unroll
                for (int n = 0; n < 2; ++n)
#pragma unroll
                    for (int j = 0; j < 8; ++j) wf[n][j] = (short)wq[(size_t)(k0 + 8 * lg + j) * HD + h * 128 + ds * 32 + n * 16 + lc];
#pragma unroll
                for (int qb = 0; qb < 2; ++qb) { const bf16x8 xf = *(const bf16x8*)(xin + (size_t)(qb32 * 32 + qb * 16 + lc) * M + k0 + 8 * lg);
                    t[qb][0] = MFMA16(wf[0], xf, t[qb][0]); t[qb][1] = MFMA16(wf[1], xf, t[qb][1]); }
            }
#pragma unroll
            for (int qb = 0; qb < 2; ++qb) { const bf16x8 f = pack2(t[qb][0] * QSCALE, t[qb][1] * QSCALE); if (ds == 0) qf[qb][0] = f; else if (ds == 1) qf[qb][1] = f; else if (ds == 2) qf[qb][2] = f; else qf[qb][3] = f; }
        }
    }
    f32x4 o[8][2] = {};
    float m[2] = {-1e30f, -1e30f}, l[2] = {0.f, 0.f};
    for (int kt = 0; kt <= qb32; ++kt) {
        f32x4 s[2][2] = {};
#pragma unroll
        for (int ds = 0; ds < 4; ++ds)
#pragma unroll
            for (int kb = 0; kb < 2; ++kb) { const bf16x8 kf = bld16c(rk, voff + (kb * 4 + ds) * 1024, (unsigned)(h * 64 + kt) * 8192u);
#pragma unroll
                for (int qb = 0; qb < 2; ++qb) s[kb][qb] = MFMA16(kf, qf[qb][ds], s[kb][qb]); }
        if (kt == qb32) attn_mask(s, 0, lane);
        bf16x8 p[2];
#pragma unroll
        for (int qb = 0; qb < 2; ++qb) {
            const float mn = __builtin_fmaxf(m[qb], rowmax8(s[0][qb], s[1][qb])), alpha = __builtin_amdgcn_exp2f(m[qb] - mn);
            m[qb] = mn;
            float ps = 0.f;
#pragma unroll
            for (int kb = 0; kb < 2; ++kb)
#pragma unroll
                for (int e = 0; e < 4; ++e) { s[kb][qb][e] = __builtin_amdgcn_exp2f(s[kb][qb][e] - mn); ps += s[kb][qb][e]; }
            l[qb] = l[qb] * alpha + ps;
#pragma unroll
            for (int db = 0; db < 8; ++db) o[db][qb] *= alpha;
            p[qb] = pack2(s[0][qb], s[1][qb]);
        }
#pragma unroll
        for (int db = 0; db < 8; ++db) { const bf16x8 v = bld16c(rv, voff + db * 1024, (unsigned)(h * 64 + kt) * 8192u); o[db][0] = MFMA16(v, p[0], o[db][0]); o[db][1] = MFMA16(v, p[1], o[db][1]); }
    }
    float il[2];
#pragma unroll
    for (int qb = 0; qb < 2; ++qb) { float t = l[qb]; t += __shfl_xor(t, 16); t += __shfl_xor(t, 32); il[qb] = __builtin_amdgcn_rcpf(t); }
    attn_store(rc, o, il, voff + ((unsigned)qb32 * 256u + h * 4) * 1024u, 0, 4);
}
#define DMA_L DMA_BC

template <int R, bool GB, int TAIL = 0  > DI void attn_phase(AttnCtx& c, const int u, f32x4 (&scur)[2][2], f32x4 (&snext)[2][2], u32x4 (&pprev)[2], u32x4 (&pnext)[2]) {
    const lds_p kp = c.fbase + ((R + 1) & 3) * 8192, vp = c.fbase + 32768 + R * 8192;
    if (!GB) {
        if (R == 0) { c.go2 += (u + 4 == c.usw) ? (unsigned)c.delta * 8192u : 0u; c.rowblk = (u == c.usw) ? c.blkP * 4 + c.wq : c.rowblk; }
        if (R == 2) c.go1 += (u + 2 == c.usw) ? (unsigned)c.delta * 8192u : 0u;
        if (R == 3) attn_loadq_if(c, u + 1);
    }
    if (UNLIKELY((c.spmask >> u) & 1)) {
        asm volatile("s_nop 7" ::: "memory");
        const int ja = u + ((!GB && u >= c.usw) ? c.delta : 0);
        attn_mask(scur, (c.rowblk - ja) * 32, c.lane);
    }
    if (R == 0 && UNLIKELY(u == 0 || (!GB && u == c.usw))) {
        asm volatile("s_nop 7" ::: "memory");
#pragma unroll
        for (int qb = 0; qb < 2; ++qb) { const float dl = __builtin_fmaxf(rowmax8(scur[0][qb], scur[1][qb]), -1e30f);
            c.mref[qb] += dl; scur[0][qb] -= dl; scur[1][qb] -= dl; const float nm = -c.mref[qb]; c.negm[qb] = f32x4{nm, nm, nm, nm}; }
    }
    PIN(c.negm[0]); PIN(c.negm[1]);
    SBAR();
    float e[16];
#define PVM(i) do { MFMA_ACC(c.o[i][0], c.fr[i], pprev[0]); MFMA_ACC(c.o[i][1], c.fr[i], pprev[1]); } while (0)
#define LSM(qb) MFMA_ACC(c.l4[qb], c.ones, pprev[qb])
#define QKM(i) do { if (TAIL == 2) break; if (((i) & 3) == 0) { MFMA_INI(snext[(i) >> 2][0], c.fr[i], c.qf[0][0], c.negm[0]); MFMA_INI(snext[(i) >> 2][1], c.fr[i], c.qf[1][0], c.negm[1]); } \
                    else { MFMA_ACC(snext[(i) >> 2][0], c.fr[i], c.qf[0][(i) & 3]); MFMA_ACC(snext[(i) >> 2][1], c.fr[i], c.qf[1][(i) & 3]); } } while (0)
#define RDK(i) do { if (TAIL != 2) RD2(i, kp); } while (0)
#define RD2(i, base) do { c.fr[(i) + 1] = *(lfrag_p)((base) + ((i) + 1) * 1024); SBAR(); c.fr[(i)] = *(lfrag_p)((base) + (i) * 1024); } while (0)
#define EX(r) do { e[r] = __builtin_amdgcn_exp2f(scur[((r) >> 2) & 1][(r) >> 3][(r) & 3]); PIN(e[r]); } while (0)
#define PK(W, i, r) do { W[i] = cvtpk(e[r], e[(r) + 1]); PIN(W); } while (0)
    PVM(0); EX(0); if (GB) SET_M0(c.kdst, ((R + 3) & 3) * 8192); else SET_M0(c.vdst, ((R + 2) & 3) * 8192); SBAR();
    PVM(1); RDK(0); EX(1); if (!TAIL) DMA_L(GB ? c.rk : c.rv, c.voff, c.go1, 0); SBAR();
    PVM(2); LSM(0); EX(2); PK(pnext[0], 0, 0); SBAR();
    PVM(3); RDK(2); EX(3); SBAR();
    PVM(4); LSM(1); EX(4); PK(pnext[0], 1, 2); SBAR();
    PVM(5); RDK(4); EX(5); if (!TAIL) DMA_L(GB ? c.rk : c.rv, c.voff, c.go1, 1024); SBAR();
    PVM(6); EX(6); PK(pnext[0], 2, 4); SBAR();
    PVM(7); RDK(6); EX(7); SBAR();
    if (GB) { if (TAIL) asm volatile("s_waitcnt vmcnt(0)\n\ts_barrier" ::: "memory"); else asm volatile("s_waitcnt vmcnt(4)\n\ts_barrier" ::: "memory"); }
    SBAR();
    QKM(0); EX(8); PK(pnext[0], 3, 6); if (GB) SET_M0(c.vdst, ((R + 3) & 3) * 8192); else SET_M0(c.kdst, R * 8192); SBAR();
    QKM(1); RD2(0, vp); EX(9); if (!TAIL) DMA_L(GB ? c.rv : c.rk, c.voff, c.go2, 0); SBAR();
    QKM(2); EX(10); PK(pnext[1], 0, 8); SBAR();
    QKM(3); RD2(2, vp); EX(11); SBAR();
    QKM(4); EX(12); PK(pnext[1], 1, 10); SBAR();
    QKM(5); RD2(4, vp); EX(13); if (!TAIL) DMA_L(GB ? c.rv : c.rk, c.voff, c.go2, 1024); SBAR();
    QKM(6); EX(14); EX(15); PK(pnext[1], 2, 12); SBAR();
    QKM(7); RD2(6, vp); PK(pnext[1], 3, 14); SBAR();
    c.go1 += 8192u; c.go2 += 8192u;
#undef EX
#undef PK
#undef PVM
#undef LSM
#undef QKM
#undef RD2
#undef RDK
    if (!GB && R == 0 && UNLIKELY(u == c.usw)) {
        float il[2];
#pragma unroll
        for (int qb = 0; qb < 2; ++qb) { il[qb] = __builtin_amdgcn_rcpf(c.l4[qb][0]); c.bad |= __ballot(!(c.l4[qb][0] <= LSUM_LIMIT)); c.l4[qb] = f32x4{}; }
        unsigned vb = c.voff + ((unsigned)(c.blkR * 8 + c.wq * 2) * 128u + c.h * 4) * 1024u;
        asm volatile("" : "+v"(vb));
        attn_store(c.rc, c.o, il, vb, 0, 4);
#pragma unroll
        for (int db = 0; db < 8; ++db) { c.o[db][0] = f32x4{}; c.o[db][1] = f32x4{}; }
    }
    PIN(pnext[0]); PIN(pnext[1]);
    SBAR();
    if (!GB) { if (TAIL) asm volatile("s_waitcnt vmcnt(0)\n\ts_barrier" ::: "memory"); else asm volatile("s_waitcnt vmcnt(6)\n\ts_barrier" ::: "memory"); }
    SBAR();
}
template <bool GB> DI void attn_stream(AttnCtx& c) {
#pragma unroll
    for (int j = 0; j < 8; ++j) c.fr[j] = *(lfrag_p)(c.fbase + j * 1024);
    SBAR();
#pragma unroll
    for (int j = 0; j < 8; ++j)
#pragma unroll
        for (int qb = 0; qb < 2; ++qb) { if ((j & 3) == 0) MFMA_INI(c.sA[j >> 2][qb], c.fr[j], c.qf[qb][0], c.negm[qb]); else MFMA_ACC(c.sA[j >> 2][qb], c.fr[j], c.qf[qb][j & 3]); SBAR(); }
    WAITV(0);
    BARRIER();
    if (!GB && c.f1flag != nullptr && c.wq == 0 && c.lane == 0) set_flag(c.f1flag, 0, c.E);
#pragma unroll
    for (int j = 0; j < 8; ++j) c.fr[j] = *(lfrag_p)(c.fbase + 32768 + 24576 + j * 1024);
    if (!GB) BARRIER();
    STAMP(c, 13);
    for (int u = 0; u < 32; u += 4) {
        STAMPL(c, 20 + u);
        attn_phase<0, GB>(c, u, c.sA, c.sB, c.pB, c.pA);
        STAMPL(c, 21 + u);
        attn_phase<1, GB>(c, u + 1, c.sB, c.sA, c.pA, c.pB);
        STAMPL(c, 22 + u);
        attn_phase<2, GB>(c, u + 2, c.sA, c.sB, c.pB, c.pA);
        STAMPL(c, 23 + u);
        attn_phase<3, GB>(c, u + 3, c.sB, c.sA, c.pA, c.pB);
    }
    STAMPL(c, 52);
    attn_phase<0, GB, 1>(c, 32, c.sA, c.sB, c.pB, c.pA);
    STAMPL(c, 53);
    attn_phase<1, GB, 2>(c, 33, c.sB, c.sA, c.pA, c.pB);
    STAMPL(c, 54);
#pragma unroll
    for (int i = 0; i < 8; ++i) { MFMA_ACC(c.o[i][0], c.fr[i], c.pB[0]); MFMA_ACC(c.o[i][1], c.fr[i], c.pB[1]); SBAR(); }
    MFMA_ACC(c.l4[0], c.ones, c.pB[0]); MFMA_ACC(c.l4[1], c.ones, c.pB[1]);
    asm volatile("s_nop 7\n\ts_nop 7" ::: "memory");
}

template <class Ctx, class KP, class F1, class F2> DI void out_phase(Ctx& c, char* lds_raw, const KP P, const int i0, const int i1, const float rsel0, const float rsel1, const unsigned E,
                                                                         F1 side_work, F2 side_publish) {
    const int tid = threadIdx.x, lane = tid & 63, lc = lane & 15, lg = lane >> 4;
    const int wid = __builtin_amdgcn_readfirstlane(tid >> 6), kh = wid >> 2, wm = (wid >> 1) & 1, wn = wid & 1;
    const int bid = blockIdx.x, xcd = bid & 7, jj = bid >> 3;
    const int rb = xcd * 4 + (jj >> 3), cb = jj & 7;
#define OUT_ROT 0
    const int rot = OUT_ROT;
    const rsrc_t rc = mk_rsrc(P->ctx, S * HD * 2), rw = mk_rsrc(P->wo, HD * M * 2), rx = mk_rsrc(P->x, S * M * 2), ro = mk_rsrc(P->out, S * M * 2);
    const unsigned voff = lane * 16, lds0 = (unsigned)(uintptr_t)(lds_p)lds_raw;
    const unsigned voffW = (lane >> 3) * 1024 + (((((lane & 7) >> 1) ^ ((lane >> 4) & 3)) << 1 | (lane & 1)) * 16);
    const unsigned cso = ((unsigned)(rb * 4 + (wid >> 1)) * 128u + (wid & 1) * 2u) * 1024u, wso = (unsigned)wid * 16384u + cb * 128u;
    const unsigned cm0 = lds0 + wid * 2048u, wm0 = lds0 + 16384u + wid * 2048u;
#define O_DMA(T, st_, j) do { const unsigned tk_ = (unsigned)(((T) + rot) & 31); \
        if ((j) == 0) { SET_M0S(cm0, st_); SBAR(); DMA_BC(rc, voff, cso + tk_ * 4096u, 0); } \
        if ((j) == 1) DMA_BC(rc, voff, cso + tk_ * 4096u, 1024); \
        if ((j) == 2) { SET_M0S(wm0, st_); SBAR(); DMA_B(rw, voffW, wso + tk_ * 131072u, 0); } \
        if ((j) == 3) { SET_M0S(wm0 + 1024u, st_); SBAR(); DMA_B(rw, voffW, wso + tk_ * 131072u + 8192u, 0); } } while (0)
#pragma unroll
    for (int T = 0; T < 4; ++T) { O_DMA(T, (unsigned)T * 32768u, 2); O_DMA(T, (unsigned)T * 32768u, 3); }
    const int orow = rb * 64 + wm * 32 + kh * 16 + lc, ocol = cb * 64 + wn * 32 + 4 * lg;
    const unsigned ooff = (unsigned)orow * 1024u + ocol * 2u;
    u32x2 xr[2];
    unsigned long long gq = __hip_atomic_load((gu64*)(P->g + orow), __ATOMIC_RELAXED, __HIP_MEMORY_SCOPE_AGENT);
#pragma unroll
    for (int nt = 0; nt < 2; ++nt) xr[nt] = __builtin_amdgcn_raw_buffer_load_b64(rx, ooff + nt * 32, 0, 0);
    side_work();
    if (wid == 0) wait_flags(P->fl, FL_CTX + (rb >> 1) * 32, 1, 32, E, lane);
    BARRIER();
    STAMP(c, 61); STAMPRT(c, 123);
#pragma unroll
    for (int T = 0; T < 4; ++T) { O_DMA(T, (unsigned)T * 32768u, 0); O_DMA(T, (unsigned)T * 32768u, 1); }
    const int tq = lc >> 2, tp = lane & 3;
    const lds_p ab = (lds_p)lds_raw + wm * 8192 + kh * 2048 + lane * 16;
    lds_p wb[2];
#pragma unroll
    for (int nt = 0; nt < 2; ++nt) wb[nt] = (lds_p)lds_raw + 16384 + (kh * 64 + 4 * lg + tq) * 128 + (((2 * wn + nt) ^ ((2 * lg + (tq >> 1)) & 3)) * 32) + 8 * tp;
    f32x4 acc[2][2] = {};
    bf16x8 af[2][2] = {}, wf[2][2] = {};
    WAITV(6); BARRIER(); STAMP(c, 62);
    side_publish();
    const int rlo = rb * 64 + wm * 32 + kh * 16;
    const bool routed = (unsigned)(i0 - rlo) < 16u || (unsigned)(i1 - rlo) < 16u;
    unsigned y3[28];
    if (UNLIKELY(routed)) {
        wait_flags(P->fl, FL_F2 + cb * 28, 1, 28, E, lane);
#pragma unroll
        for (int k = 0; k < 28; ++k) y3[k] = ld_agent(P->p2 + (k * 2 + (lane >> 5)) * M + cb * 64 + wn * 32 + (lane & 31));
        WAITV(0);
    }
#define O_MMA(buf) do { _Pragma("unroll") for (int q = 0; q < 4; ++q) { acc[q & 1][q >> 1] = MFMA16(wf[buf][q & 1], af[buf][q >> 1], acc[q & 1][q >> 1]); PIN(acc[q & 1][q >> 1]); } } while (0)
#define O_RD(buf, st, sl) do { _Pragma("unroll") for (int q = 0; q < 2; ++q) { af[buf][q] = *(lfrag_p)(ab + (st) + q * 4096 + (sl) * 1024); \
        wf[buf][q] = cat8(trd(wb[q] + (st) + (sl) * 4096), trd(wb[q] + (st) + (sl) * 4096 + 2048)); } } while (0)
#define O_M1(buf, q) do { acc[(q) & 1][(q) >> 1] = MFMA16(wf[buf][(q) & 1], af[buf][(q) >> 1], acc[(q) & 1][(q) >> 1]); PIN(acc[(q) & 1][(q) >> 1]); } while (0)
#define O_RA(buf, st, sl, q) af[buf][q] = *(lfrag_p)(ab + (st) + (q) * 4096 + (sl) * 1024)
#define O_RW(buf, st, sl, q) wf[buf][q] = cat8(trd(wb[q] + (st) + (sl) * 4096), trd(wb[q] + (st) + (sl) * 4096 + 2048))
    unsigned st = 0u, sd = 4u * 32768u;
#pragma unroll 1
    for (int T = 0; T < 32; ++T) {
        const bool more = T + 4 < 32;
        O_M1(1, 0); O_RA(0, st, 0, 0); SBAR(); O_M1(1, 1); O_RW(0, st, 0, 0); SBAR(); O_M1(1, 2); O_RA(0, st, 0, 1); SBAR(); O_M1(1, 3); O_RW(0, st, 0, 1); if (more) { O_DMA(T + 4, sd, 0); O_DMA(T + 4, sd, 1); } SBAR();
        O_M1(0, 0); O_RA(1, st, 1, 0); SBAR(); O_M1(0, 1); O_RW(1, st, 1, 0); SBAR(); O_M1(0, 2); O_RA(1, st, 1, 1); SBAR(); O_M1(0, 3); O_RW(1, st, 1, 1); if (more) { O_DMA(T + 4, sd, 2); O_DMA(T + 4, sd, 3); } SBAR();
        if (T < 3) { if (T == 0) WAITV(8); else if (T == 1) WAITV(10); else WAITV(12); } else if (more) WAITV(12); else if (T == 28) WAITV(8); else if (T == 29) WAITV(4); else WAITV(0);
        BARRIER();
        sd = st; st = st == 4u * 32768u ? 0u : st + 32768u;
    }
    O_MMA(1);
    STAMP(c, 63); STAMPRT(c, 124);
    {
        const lds_p xw = (lds_p)lds_raw + wid * 2048 + lane * 16, xq = (lds_p)lds_raw + (wid ^ 4) * 2048 + lane * 16;
        *(lf4_p)(xw) = kh ? acc[0][0] : acc[0][1]; *(lf4_p)(xw + 1024) = kh ? acc[1][0] : acc[1][1];
        BARRIER();
        f32x4 v[2];
        v[0] = (kh ? acc[0][1] : acc[0][0]) + *(lf4_p)(xq); v[1] = (kh ? acc[1][1] : acc[1][0]) + *(lf4_p)(xq + 1024);
        float yv = 0.f;
        if (UNLIKELY(routed)) {
            float t = 0.f;
#pragma unroll
            for (int k = 0; k < 28; ++k) t += __uint_as_float(y3[k]);
            yv = rbf(rbf(t) * (lane < 32 ? rsel0 : rsel1));
        }
        { unsigned spins = 0; while (__any((unsigned)(gq >> 32) != E) && ++spins < (1u << 20)) gq = __hip_atomic_load((gu64*)(P->g + orow), __ATOMIC_RELAXED, __HIP_MEMORY_SCOPE_AGENT); }
        const float gg = __uint_as_float((unsigned)gq);
        const float omg = rbf(1.f - gg);
#pragma unroll
        for (int nt = 0; nt < 2; ++nt) {
            float b[4], o[4];
#pragma unroll
            for (int e = 0; e < 4; ++e) { b[e] = __uint_as_float((e & 1) ? (xr[nt][e >> 1] & 0xffff0000u) : (xr[nt][e >> 1] << 16)); b[e] = b[e] + b[e] * 0.f; }
            if (UNLIKELY(routed)) {
#pragma unroll
                for (int e = 0; e < 4; ++e) { const int cl = 4 * lg + 16 * nt + e; const float y0 = __shfl(yv, cl), y1 = __shfl(yv, 32 + cl);
                    if (orow == i0) b[e] = rbf(b[e] + y0); if (orow == i1) b[e] = rbf(b[e] + y1); }
            }
#pragma unroll
            for (int e = 0; e < 4; ++e) o[e] = rbf(gg * rbf(v[nt][e])) + rbf(omg * b[e]);
            u32x2 w = {cvtpk(o[0], o[1]), cvtpk(o[2], o[3])};
            __builtin_amdgcn_raw_buffer_store_b64(w, ro, ooff + nt * 32, 0, 0);
        }
    }
}

template <class Ctx, class KP, class F1, class F2> DI void out_phase_sk(Ctx& c, char* lds_raw, const KP P, const int i0, const int i1, const float rsel0, const float rsel1, const unsigned E,
                                                                        F1 side_work, F2 side_publish) {
    const int tid = threadIdx.x, lane = tid & 63, lc = lane & 15, lg = lane >> 4;
    const int wid = __builtin_amdgcn_readfirstlane(tid >> 6), wm = wid >> 2, wn = wid & 3;
    const int bid = blockIdx.x, xcd = bid & 7, jj = bid >> 3;
    const int ks = xcd & 3, tile = (xcd >> 2) * 32 + jj, rb = tile >> 2, cb = tile & 3;
    const rsrc_t rc = mk_rsrc(P->ctx, S * HD * 2), rw = mk_rsrc(P->wo, HD * M * 2), rx = mk_rsrc(P->x, S * M * 2), rs = mk_rsrc(P->slab, 64 * 16 * 16384), ro = mk_rsrc(P->out, S * M * 2);
    const unsigned voff = lane * 16, lds0 = (unsigned)(uintptr_t)(lds_p)lds_raw;
    unsigned voffW[2];
#pragma unroll
    for (int par = 0; par < 2; ++par) voffW[par] = (lane >> 4) * 1024 + (((lane & 15) ^ ((4 * par + (lane >> 4)) << 1)) * 16);
    const unsigned cso = ((unsigned)(rb * 8 + wid) * 128u + ks * 32u) * 1024u, wso = ((unsigned)ks * 1024u + wid * 8u) * 1024u + cb * 256u;
    const unsigned cm0 = lds0 + wid * 2048u, wm0 = lds0 + 16384u + wid * 2048u;
#define DMA_X DMA_BC
#define S_DMA(T, st_, j) do { \
        if ((j) == 0) { SET_M0S(cm0, st_); SBAR(); DMA_X(rc, voff, cso + (unsigned)(T) * 2048u, 0); } \
        if ((j) == 1) DMA_X(rc, voff, cso + (unsigned)(T) * 2048u, 1024); \
        if ((j) == 2) { SET_M0S(wm0, st_); SBAR(); DMA_B(rw, voffW[0], wso + (unsigned)(T) * 65536u, 0); } \
        if ((j) == 3) { SET_M0S(wm0 + 1024u, st_); SBAR(); DMA_B(rw, voffW[1], wso + (unsigned)(T) * 65536u + 4096u, 0); } } while (0)
#pragma unroll
    for (int T = 0; T < 4; ++T) { S_DMA(T, (unsigned)T * 32768u, 2); S_DMA(T, (unsigned)T * 32768u, 3); }
    const bool fin = wm == (ks >> 1);
    const int orow = rb * 128 + ks * 32 + lc, ocol = cb * 128 + wn * 32 + 4 * lg;
    const unsigned ooff = (unsigned)orow * 1024u + ocol * 2u;
    u32x2 xr[2][2]; unsigned long long gq[2];
#pragma unroll
    for (int j = 0; j < 2; ++j) { gq[j] = __hip_atomic_load((gu64*)(P->g + orow + 16 * j), __ATOMIC_RELAXED, __HIP_MEMORY_SCOPE_AGENT);
#pragma unroll
        for (int nt = 0; nt < 2; ++nt) xr[j][nt] = __builtin_amdgcn_raw_buffer_load_b64(rx, ooff + j * 16384 + nt * 32, 0, 0); }
    side_work();
    if (wid == 0) wait_flags(P->fl, FL_CTX + rb * 32 + ks * 8, 1, 8, E, lane);
    BARRIER();
    STAMP(c, 61); STAMPRT(c, 123);
#pragma unroll
    for (int T = 0; T < 4; ++T) { S_DMA(T, (unsigned)T * 32768u, 0); S_DMA(T, (unsigned)T * 32768u, 1); }
    const int tq = lc >> 2, tp = lane & 3, tsw = 4 * (lg & 1) + tq;
    const lds_p ab = (lds_p)lds_raw + wm * 8192 + lane * 16;
    lds_p wb[2];
#pragma unroll
    for (int nt = 0; nt < 2; ++nt) wb[nt] = (lds_p)lds_raw + 16384 + (4 * lg + tq) * 256 + (((((2 * wn + nt) ^ tsw) << 1) | (tp >> 1)) * 16) + 8 * (tp & 1);
    f32x4 acc[2][4] = {};
    bf16x8 af[2][4] = {}, wf[2][2] = {};
    WAITV(6); BARRIER(); STAMP(c, 62);
    side_publish();
    const int rlo = rb * 128 + ks * 32;
    const bool routed = fin && ((unsigned)(i0 - rlo) < 32u || (unsigned)(i1 - rlo) < 32u);
    unsigned y3[28];
    if (UNLIKELY(routed)) {
        wait_flags(P->fl, FL_F2 + (2 * cb + (wn >> 1)) * 28, 1, 28, E, lane);
#pragma unroll
        for (int k = 0; k < 28; ++k) y3[k] = ld_agent(P->p2 + (k * 2 + (lane >> 5)) * M + cb * 128 + wn * 32 + (lane & 31));
        WAITV(0);
    }
#define S_MMA(buf, mt, nt) do { acc[nt][mt] = MFMA16(wf[buf][nt], af[buf][mt], acc[nt][mt]); PIN(acc[nt][mt]); } while (0)
#define S_RDA(buf, mt, st, s) af[buf][mt] = *(lfrag_p)(ab + (st) + (mt) * 2048 + (s) * 1024)
#define S_RDW(buf, nt, st, s) wf[buf][nt] = cat8(trd(wb[nt] + (st) + (s) * 8192), trd(wb[nt] + (st) + (s) * 8192 + 4096))
#define S_HALF(mb, rb_, s, D0, D1) do { \
        S_MMA(mb, 0, 0); S_RDW(rb_, 0, st, s); SBAR(); S_MMA(mb, 0, 1); S_RDA(rb_, 0, st, s); SBAR(); S_MMA(mb, 1, 0); S_RDW(rb_, 1, st, s); SBAR(); S_MMA(mb, 1, 1); S_RDA(rb_, 1, st, s); SBAR(); \
        S_MMA(mb, 2, 0); S_RDA(rb_, 2, st, s); SBAR(); S_MMA(mb, 2, 1); if (more) S_DMA(T + 4, sd, D0); SBAR(); S_MMA(mb, 3, 0); S_RDA(rb_, 3, st, s); SBAR(); S_MMA(mb, 3, 1); if (more) S_DMA(T + 4, sd, D1); SBAR(); } while (0)
    unsigned st = 0u, sd = 4u * 32768u;
#pragma unroll
    for (int T = 0; T < 16; ++T) {
        const bool more = T + 4 < 16;
        S_HALF(1, 0, 0, 0, 1);
        S_HALF(0, 1, 1, 2, 3);
        if (T < 3) { if (T == 0) WAITV(8); else if (T == 1) WAITV(10); else WAITV(12); } else if (more) WAITV(12); else if (T == 12) WAITV(8); else if (T == 13) WAITV(4); else WAITV(0);
        BARRIER();
        sd = st; st = st == 4u * 32768u ? 0u : st + 32768u;
    }
#pragma unroll
    for (int mt = 0; mt < 4; ++mt) { S_MMA(1, mt, 0); S_MMA(1, mt, 1); }
    STAMP(c, 63); STAMPRT(c, 124);
#pragma unroll
    for (int j = 0; j < 2; ++j) {
        const int dst = 2 * wm + j;
        if (dst != ks) {
            const unsigned so = (unsigned)(((tile * 4 + dst) * 4 + ks) * 4 + wn) * 4096u + voff;
#pragma unroll
            for (int q = 0; q < 4; ++q) __builtin_amdgcn_raw_buffer_store_b128(__builtin_bit_cast(u32x4, acc[q & 1][2 * j + (q >> 1)]), rs, so + q * 1024, 0, 16);
        }
    }
    WAITV(0);
    if (lane < 2 && 2 * wm + lane != ks) set_flag(P->fl, FL_OUT + ((tile * 4 + 2 * wm + lane) * 4 + ks) * 4 + wn, E);
    STAMP(c, 64);
    if (wid == (ks >> 1) * 4) {
        unsigned spins = 0;
        while (__ballot(lane < 16 && (lane >> 2) != ks && ld_agent(P->fl + 32 * (FL_OUT + (tile * 4 + ks) * 16 + (lane & 15))) != E) != 0ull && ++spins < (1u << 22)) __builtin_amdgcn_s_sleep(1);
    }
    BARRIER();
    STAMP(c, 65); STAMPRT(c, 125);
    if (fin) {
        f32x4 v[4];
#pragma unroll
        for (int q = 0; q < 4; ++q) v[q] = (ks & 1) ? acc[q & 1][2 + (q >> 1)] : acc[q & 1][q >> 1];
        u32x4 pt[3][4];
#pragma unroll
        for (int j = 0; j < 3; ++j) { const int src = j + (j >= ks ? 1 : 0); const unsigned so = (unsigned)(((tile * 4 + ks) * 4 + src) * 4 + wn) * 4096u + voff;
#pragma unroll
            for (int q = 0; q < 4; ++q) pt[j][q] = __builtin_amdgcn_raw_buffer_load_b128(rs, so + q * 1024, 0, 16); }
        float yv = 0.f;
        if (UNLIKELY(routed)) {
            float t = 0.f;
#pragma unroll
            for (int k = 0; k < 28; ++k) t += __uint_as_float(y3[k]);
            yv = rbf(rbf(t) * (lane < 32 ? rsel0 : rsel1));
        }
        { unsigned spins = 0; while (__any((unsigned)(gq[0] >> 32) != E || (unsigned)(gq[1] >> 32) != E) && ++spins < (1u << 20)) {
              gq[0] = __hip_atomic_load((gu64*)(P->g + orow), __ATOMIC_RELAXED, __HIP_MEMORY_SCOPE_AGENT); gq[1] = __hip_atomic_load((gu64*)(P->g + orow + 16), __ATOMIC_RELAXED, __HIP_MEMORY_SCOPE_AGENT); } }
#pragma unroll
        for (int j = 0; j < 3; ++j)
#pragma unroll
            for (int q = 0; q < 4; ++q) v[q] += __builtin_bit_cast(f32x4, pt[j][q]);
#pragma unroll
        for (int q = 0; q < 4; ++q) {
            const int j = q >> 1, nt = q & 1, row = orow + 16 * j;
            const float gg = __uint_as_float((unsigned)gq[j]), omg = rbf(1.f - gg);
            float b[4], o[4];
#pragma unroll
            for (int e = 0; e < 4; ++e) { b[e] = __uint_as_float((e & 1) ? (xr[j][nt][e >> 1] & 0xffff0000u) : (xr[j][nt][e >> 1] << 16)); b[e] = b[e] + b[e] * 0.f; }
            if (UNLIKELY(routed)) {
#pragma unroll
                for (int e = 0; e < 4; ++e) { const int cl = 4 * lg + 16 * nt + e; const float y0 = __shfl(yv, cl), y1 = __shfl(yv, 32 + cl);
                    if (row == i0) b[e] = rbf(b[e] + y0); if (row == i1) b[e] = rbf(b[e] + y1); }
            }
#pragma unroll
            for (int e = 0; e < 4; ++e) o[e] = rbf(gg * rbf(v[q][e])) + rbf(omg * b[e]);
            u32x2 w = {cvtpk(o[0], o[1]), cvtpk(o[2], o[3])};
            __builtin_amdgcn_raw_buffer_store_b64(w, ro, ooff + j * 16384 + nt * 32, 0, 0);
        }
    }
}

DI int ffn_swz(int k) { return ((k >> 1) & 1) | (((k >> 3) & 1) << 1); }
DI int ffn_tr_base(int lane, int nt) { const int g = lane >> 4, q = (lane & 15) >> 2, p = lane & 3, k = 8 * g + q; return k * 128 + ((nt ^ ffn_swz(k)) * 32) + 8 * p; }
struct Params {
    const u16 *x, *ckv, *wq, *wk, *wv, *wo, *wr, *wg, *wu, *wd, *gw;
    u16 *Kf, *Vf, *ctx, *out;
    unsigned long long *r, *g;
    float *p1, *p2, *slab;
    unsigned* fl;
    unsigned long long* dbg;
};
typedef __attribute__((address_space(4))) const Params* kargs_t;
DI kargs_t kargs() { kargs_t p = (kargs_t)__builtin_amdgcn_kernarg_segment_ptr(); asm volatile("" : "+s"(p)); return p; }
__global__ __launch_bounds__(512, 2) void fused_kernel(const Params) {
    extern __shared__ __attribute__((aligned(16))) char lds_raw[];
    const int tid = threadIdx.x, lane = tid & 63, lc = lane & 15, lg = lane >> 4;
    const int wid = __builtin_amdgcn_readfirstlane(tid >> 6);
    const int bid = blockIdx.x, xcd = bid & 7, jj = bid >> 3;
    const int h = xcd * 4 + (jj >> 3), x = jj & 7;
    ProjCtx c;
    c.wid = wid; c.lane = lane; c.rot = PROJ_ROT;
    STAMP_BEGIN(c);
    unsigned E;
    unsigned warm_ = 0u;
    u32x4 sx, sw, sg;
    { const kargs_t P = kargs();
      c.ra1 = mk_rsrc(P->ckv, S * M * 2); c.ra2 = mk_rsrc(P->x, S * M * 2); c.rwk = mk_rsrc(P->wk, M * HD * 2); c.rwv = mk_rsrc(P->wv, M * HD * 2); c.rwq = mk_rsrc(P->wq, M * HD * 2);
      asm volatile("buffer_load_dwordx4 %0, %1, %2, 0 offen" : "=v"(sx) : "v"((unsigned)(bid * 8 + wid) * (M * 2) + lane * 16u), "s"(c.ra2) : "memory");
      asm volatile("buffer_load_dwordx4 %0, %1, %2, 0 offen" : "=v"(sw) : "v"(lane * 16u), "s"(mk_rsrc(P->wr, M * 2)) : "memory");
      asm volatile("buffer_load_dwordx4 %0, %1, %2, 0 offen" : "=v"(sg) : "v"(lane * 16u), "s"(mk_rsrc(P->gw, M * 2)) : "memory");
      asm volatile("buffer_load_dword %0, %1, %2, 0 offen sc1" : "=v"(E) : "v"(0u), "s"(mk_rsrc(P->fl, 4)) : "memory"); }
#pragma unroll
    for (int par = 0; par < 2; ++par) c.voffA[par] = (lane >> 3) * 1024 + (((lane & 7) ^ ((4 * par + (lane >> 4)) & 7)) * 16);
    c.voffW = (lane >> 4) * 8192 + (((lane & 15) ^ (((lane >> 4) | ((wid & 1) << 2)) << 1)) * 16);
    c.lds0 = (unsigned)(uintptr_t)(lds_p)lds_raw;
    c.arow = (unsigned)x * 256u * 1024u; c.arow2 = (unsigned)(wid < 4 ? x * 128 : (15 - x) * 128 - 128) * 1024u; c.wcol = (unsigned)h * 256u;
    const bool ffw = jj < 28;
    proj_dma_a<0>(c, false, 0, 0u); proj_dma_a<1>(c, false, 0, 0u); proj_dma_a<2>(c, false, 0, 0u); proj_dma_a<3>(c, false, 0, 0u);
    proj_dma_w<0>(c, c.rwk, 0); proj_dma_w<1>(c, c.rwk, 0); proj_dma_w<2>(c, c.rwv, 0); proj_dma_w<3>(c, c.rwv, 0);
    proj_dma_a<0>(c, false, 1, 32768u); proj_dma_a<1>(c, false, 1, 32768u); proj_dma_a<2>(c, false, 1, 32768u); proj_dma_a<3>(c, false, 1, 32768u);
    {
        asm volatile("s_waitcnt vmcnt(12)" : "+v"(sx), "+v"(sw), "+v"(sg), "+v"(E), "+v"(warm_) :: "memory");
        E = __builtin_amdgcn_readfirstlane(E) + 1u;
        const kargs_t P = kargs();
        float sr = 0.f, sgs = 0.f;
#pragma unroll
        for (int j = 0; j < 8; ++j) { const float xv = __uint_as_float((j & 1) ? (sx[j >> 1] & 0xffff0000u) : (sx[j >> 1] << 16));
            sr += xv * __uint_as_float((j & 1) ? (sw[j >> 1] & 0xffff0000u) : (sw[j >> 1] << 16)); sgs += rbf(xv * __uint_as_float((j & 1) ? (sg[j >> 1] & 0xffff0000u) : (sg[j >> 1] << 16))); }
        sr = wave_sum(sr); sgs = wave_sum(sgs);
        const int srow = bid * 8 + wid;
        if (lane == 0) { __hip_atomic_store((gu64*)(P->r + srow), ((unsigned long long)E << 32) | __float_as_uint(rbf(sr)), __ATOMIC_RELAXED, __HIP_MEMORY_SCOPE_AGENT);
                         __hip_atomic_store((gu64*)(P->g + srow), ((unsigned long long)E << 32) | __float_as_uint(rbf(1.f / (1.f + __expf(-rbf(sgs))))), __ATOMIC_RELAXED, __HIP_MEMORY_SCOPE_AGENT); }
    }
    float rsel0 = 0.f, rsel1 = 0.f; int ti1 = 0, ti2 = 0;
    unsigned m1 = 0u, m2 = 0u;
    const unsigned voff = lane * 16;
    const int wm = wid >> 2, wn = wid & 3, wm2 = wid >> 1, wn2 = wid & 1;
    const bool isK = wn < 2;
    const int tq = lc >> 2, tp = lane & 3, tsw = tq | ((lg & 1) << 2);
    lds_p ab[2], wb[4];
#define PROJ_BASES(row0, b16_0, woff) do { \
        _Pragma("unroll") for (int s = 0; s < 2; ++s) ab[s] = (lds_p)lds_raw + ((row0) + lc) * 128 + (((4 * s + lg) ^ ((lc >> 1) & 7)) * 16); \
        _Pragma("unroll") for (int nt = 0; nt < 4; ++nt) wb[nt] = (lds_p)lds_raw + 98304 + (woff) + (8 * lg + tq) * 256 + ((((((b16_0) + nt) ^ tsw) << 1) | (tp >> 1)) * 16) + 8 * (tp & 1); } while (0)
    PROJ_BASES(wm * 128, 4 * (wn & 1), isK ? 0 : 16384);
#define AFR(sl, s, mt) (*(lfrag_p)(ab[s] + (sl) + (mt) * 2048))
#define WFR(sl, s, nt) cat8(trd(wb[nt] + (sl) + (s) * 8192), trd(wb[nt] + (sl) + (s) * 8192 + 1024))
    f32x4 acc[4][8] = {};
    bf16x8 af[4] = {}, wf[2][4] = {};
    WAITV(6); STAMP(c, 1); BARRIER(); STAMP(c, 2); STAMPRT(c, 118);
    unsigned asr = 0u, asr1 = 32768u, asr2 = 65536u;
    unsigned asd = 65536u, asd1 = 0u, asd2 = 32768u;
#define ROT() do { { const unsigned t_ = asr; asr = asr1; asr1 = asr2; asr2 = t_; } { const unsigned t_ = asd; asd = asd1; asd1 = asd2; asd2 = t_; } } while (0)
#define P1_GRP(ISK, wbuf, mt, ai) do { _Pragma("unroll") for (int nt = 0; nt < 4; ++nt) { \
        if (ISK) acc[nt][mt] = MFMA16(wf[wbuf][nt], af[ai], acc[nt][mt]); else acc[nt][mt] = MFMA16(af[ai], wf[wbuf][nt], acc[nt][mt]); PIN(acc[nt][mt]); } } while (0)
#define P1_MM(ISK, wbuf, mt, ai, nt) do { if (ISK) acc[nt][mt] = MFMA16(wf[wbuf][nt], af[ai], acc[nt][mt]); else acc[nt][mt] = MFMA16(af[ai], wf[wbuf][nt], acc[nt][mt]); PIN(acc[nt][mt]); } while (0)
#define P1_INT(ISK) do { \
        const unsigned ws_ = (unsigned)(t & 1) * 32768u; \
        const bool LAST = t == 7, A2 = t >= 6; \
        const rsrc_t rw1 = LAST ? c.rwq : c.rwk, rw2 = LAST ? rnull : c.rwv;        \
          \
        _Pragma("unroll") for (int p = 0; p < 16; ++p) { \
            const int wb_ = (p >= 3 && p < 11) ? 0 : 1, mt_ = p < 3 ? 5 + p : (p < 11 ? p - 3 : p - 11); \
            P1_MM(ISK, wb_, mt_, p & 3, 0); af[(p + 3) & 3] = AFR(asr, p >> 3, p & 7); SBAR(); \
            P1_MM(ISK, wb_, mt_, p & 3, 1); if (p < 2) wf[0][2 * p] = WFR(ws_, 0, 2 * p); if (p >= 4 && p < 8) wf[1][p - 4] = WFR(ws_, 1, p - 4); SBAR(); \
            P1_MM(ISK, wb_, mt_, p & 3, 2); if (p < 2) wf[0][2 * p + 1] = WFR(ws_, 0, 2 * p + 1); SBAR(); \
            P1_MM(ISK, wb_, mt_, p & 3, 3); \
            if (p == 1) proj_dma_w<0>(c, rw1, t + 1); if (p == 3) proj_dma_w<1>(c, rw1, t + 1); \
            if (p == 5) proj_dma_w<2>(c, rw2, t + 1); if (p == 7) proj_dma_w<3>(c, rw2, t + 1); \
            if (p == 9) proj_dma_a<0>(c, A2, t + 2, asd); if (p == 10) proj_dma_a<1>(c, A2, t + 2, asd); \
            if (p == 12) proj_dma_a<2>(c, A2, t + 2, asd); if (p == 13) proj_dma_a<3>(c, A2, t + 2, asd); \
            if (p == 14 && t == 0) TOUCH(); \
            SBAR(); } \
        STAMPP(c, 102 + t); if (t == 0) WAITV(5); else WAITV(4); \
        BARRIER(); STAMPP(c, 70 + t); ROT(); } while (0)
    unsigned dummy_ = 0u;
#define TOUCH() do { const kargs_t P_ = kargs(); \
        const void* tp_ = wid < 2 ? (const void*)P_->wo : (wid < 4 ? (const void*)P_->wq : (wid < 6 ? (const void*)P_->x : (const void*)P_->wd)); \
        const unsigned tn_ = wid < 4 ? HD * M * 2 : (wid == 4 ? S * M * 2 : ((ffw && wid == 6) ? F * M * 2 : 0)); \
        const unsigned to_ = wid < 4 ? (unsigned)bid * 16384u + (unsigned)(wid & 1) * 8192u + lane * 128u : (wid == 4 ? (unsigned)bid * 8192u + lane * 128u : (unsigned)(jj * 64 + lane) * (M * 2) + xcd * 128u); \
        asm volatile("buffer_load_dword %0, %1, %2, 0 offen" : "+v"(dummy_) : "v"(to_), "s"(mk_rsrc(tp_, tn_)) : "memory"); } while (0)
    const rsrc_t rnull = mk_rsrc(nullptr, 0);
    bf16x8 pk[16];
#define P1_ALL(ISK) do { { const int t = 0; P1_INT(ISK); } _Pragma("unroll 1") for (int t = 1; t < 6; ++t) P1_INT(ISK); { const int t = 6; P1_INT(ISK); } { const int t = 7; P1_INT(ISK); } \
        PROJ_BASES(wm2 * 64, 4 * wn2, 0); \
          \
        _Pragma("unroll") for (int p = 0; p < 3; ++p) { P1_GRP(ISK, 1, 5 + p, p & 3); af[(p + 3) & 3] = AFR(asr, p >> 2, p & 3); \
            if (p < 2) { wf[0][2 * p] = WFR(0u, 0, 2 * p); wf[0][2 * p + 1] = WFR(0u, 0, 2 * p + 1); } \
            if (p == 0) proj_dma_w<0>(c, c.rwq, 9); if (p == 2) proj_dma_w<1>(c, c.rwq, 9); SBAR(); } \
        _Pragma("unroll") for (int i = 0; i < 16; ++i) { \
            pk[i] = (ISK) ? pack2(acc[2 * (i >> 3)][i & 7], acc[2 * (i >> 3) + 1][i & 7]) : pack2(acc[i >> 2][2 * (i & 3)], acc[i >> 2][2 * (i & 3) + 1]); PIN(pk[i]); } \
        SBAR(); } while (0)
    if (isK) P1_ALL(true); else P1_ALL(false);
    asm volatile("" :: "v"(dummy_));
    const rsrc_t rKV = mk_rsrc(isK ? kargs()->Kf : kargs()->Vf, S * HD * 2);
    const unsigned kvbase = voff + (unsigned)(h * 64 + x * 8 + wm * 4) * 8192u + (unsigned)(wn & 1) * (isK ? 2048u : 4096u);
#define KV_OUT(i) bst16_wt(rKV, pk[i], kvbase, isK ? (unsigned)((((i) & 7) >> 1) * 8192 + ((i) & 1) * 4096 + ((i) >> 3) * 1024) : (unsigned)(((i) & 3) * 8192 + ((i) >> 2) * 1024))
    f32x4 acq[4][4] = {};
    u32x4 ra_ = {}, rb_ = {};
#define TOP2_STEP(O1, O2) do { const unsigned o1_ = (O1), o2_ = (O2); m2 = max(min(m1, o1_), max(m2, o2_)); m1 = max(m1, o1_); } while (0)
#define P2_GRP(wbuf, mt, ai) do { _Pragma("unroll") for (int nt = 0; nt < 4; ++nt) { acq[nt][mt] = MFMA16(wf[wbuf][nt], af[ai], acq[nt][mt]); PIN(acq[nt][mt]); } } while (0)
#define P2_MM(wbuf, mt, ai, nt) do { acq[nt][mt] = MFMA16(wf[wbuf][nt], af[ai], acq[nt][mt]); PIN(acq[nt][mt]); } while (0)
#define P2_POS(p, ws_) do { const int wb_ = ((p) >= 3 && (p) < 7) ? 0 : 1, mt_ = (p) < 3 ? 1 + (p) : ((p) < 7 ? (p) - 3 : 0); \
        P2_MM(wb_, mt_, (p) & 3, 0); af[((p) + 3) & 3] = AFR(asr, (p) >> 2, (p) & 3); SBAR(); \
        P2_MM(wb_, mt_, (p) & 3, 1); if ((p) < 2) wf[0][2 * (p)] = WFR(ws_, 0, 2 * (p)); if ((p) == 3 || (p) == 4) wf[1][2 * ((p) - 3)] = WFR(ws_, 1, 2 * ((p) - 3)); SBAR(); \
        P2_MM(wb_, mt_, (p) & 3, 2); if ((p) < 2) wf[0][2 * (p) + 1] = WFR(ws_, 0, 2 * (p) + 1); if ((p) == 3 || (p) == 4) wf[1][2 * ((p) - 3) + 1] = WFR(ws_, 1, 2 * ((p) - 3) + 1); SBAR(); \
        P2_MM(wb_, mt_, (p) & 3, 3); } while (0)
    {
        P2_POS(3, 0u); proj_dma_a<0>(c, true, 10, asd); SBAR(); P2_POS(4, 0u); proj_dma_a<1>(c, true, 10, asd); SBAR();
        P2_POS(5, 0u); proj_dma_a<2>(c, true, 10, asd); SBAR(); P2_POS(6, 0u); proj_dma_a<3>(c, true, 10, asd); SBAR(); P2_POS(7, 0u); SBAR();
        WAITV(4); BARRIER(); ROT();
    }
#pragma unroll
    for (int t = 9; t < 16; ++t) {
        const unsigned ws_ = (unsigned)(t & 1) * 32768u;
        const bool mw = t + 1 < 16, ma = t + 2 < 16;
        if (t == 11) {
            const unsigned* sk = (const unsigned*)(lds_raw + 147456);
            m1 = 0u; m2 = 0u;
#pragma unroll
            for (int w = 0; w < 8; ++w) TOP2_STEP(sk[2 * w], sk[2 * w + 1]);
            const int i1 = 2047 - (int)(m1 & 2047u), i2 = 2047 - (int)(m2 & 2047u);
            const unsigned o1 = m1 >> 16, o2 = m2 >> 16;
            const float v1 = __uint_as_float(((o1 & 0x8000u) ? (o1 & 0x7fffu) : (~o1 & 0xffffu)) << 16), v2 = __uint_as_float(((o2 & 0x8000u) ? (o2 & 0x7fffu) : (~o2 & 0xffffu)) << 16);
            rsel0 = rbf(1.f / (1.f + __expf(-v1))); rsel1 = rbf(1.f / (1.f + __expf(-v2)));
            ti1 = __builtin_amdgcn_readfirstlane(i1); ti2 = __builtin_amdgcn_readfirstlane(i2);
        }
        P2_POS(0, ws_); if (mw) proj_dma_w<0>(c, c.rwq, t + 1); SBAR();
        P2_POS(1, ws_); if (mw) proj_dma_w<1>(c, c.rwq, t + 1); SBAR();
        P2_POS(2, ws_); if (ma) proj_dma_a<0>(c, true, t + 2, asd); SBAR();
        P2_POS(3, ws_); if (ma) proj_dma_a<1>(c, true, t + 2, asd); SBAR();
        P2_POS(4, ws_); if (ma) proj_dma_a<2>(c, true, t + 2, asd); SBAR();
        P2_POS(5, ws_); if (ma) proj_dma_a<3>(c, true, t + 2, asd); SBAR();
        P2_POS(6, ws_);
        if (t == 10) {
            const rsrc_t rgu = mk_rsrc(wid < 4 ? kargs()->wg : kargs()->wu, ffw ? M * F * 2 : 0);
#pragma unroll
            for (int j = 0; j < 2; ++j) { const int krow = (wid & 3) * 16 + j * 8 + (lane >> 3);
                const unsigned vo = (unsigned)(xcd * 64 + krow) * (F * 2) + jj * 128u + ((((((lane & 7) >> 1) ^ ffn_swz(krow)) << 1) | (lane & 1)) * 16u);
                SET_M0(c.lds0 + 114688u + wid * 2048u, j * 1024); SBAR(); DMA_B(rgu, vo, 0u, 0); } }
        SBAR();
        P2_POS(7, ws_); SBAR();
        switch (t) { case 9: KV_OUT(0); KV_OUT(1); KV_OUT(2); break; case 10: KV_OUT(3); KV_OUT(4); KV_OUT(5); break; case 11: KV_OUT(6); KV_OUT(7); KV_OUT(8); break;
                     case 12: KV_OUT(9); KV_OUT(10); KV_OUT(11); break; case 13: KV_OUT(12); KV_OUT(13); break; case 14: KV_OUT(14); KV_OUT(15); break; default: break; }
        if (t == 9) {
            const rsrc_t rr = mk_rsrc(kargs()->r, S * 8);
            asm volatile("buffer_load_dwordx4 %0, %1, %2, 0 offen sc1" : "=v"(ra_) : "v"(tid * 32u), "s"(rr) : "memory");
            asm volatile("buffer_load_dwordx4 %0, %1, %2, 0 offen offset:16 sc1" : "=v"(rb_) : "v"(tid * 32u), "s"(rr) : "memory");
        }
        if (t == 10) {
            unsigned* sk = (unsigned*)(lds_raw + 147456);
            asm volatile("s_waitcnt vmcnt(9)" : "+v"(ra_), "+v"(rb_) :: "memory");
            { unsigned spins = 0;
              while (__any(ra_[1] != E || ra_[3] != E || rb_[1] != E || rb_[3] != E) && ++spins < (1u << 20)) {
                  const rsrc_t rr = mk_rsrc(kargs()->r, S * 8);
                  ra_ = __builtin_amdgcn_raw_buffer_load_b128(rr, tid * 32u, 0, 16); rb_ = __builtin_amdgcn_raw_buffer_load_b128(rr, tid * 32u + 16u, 0, 16); } }
            const u32x4 r4 = {ra_[0], ra_[2], rb_[0], rb_[2]};
#pragma unroll
            for (int e = 0; e < 4; ++e) { const unsigned b16 = r4[e] >> 16, ord = (b16 & 0x8000u) ? (~b16 & 0xffffu) : (b16 | 0x8000u);
                const unsigned key = ((b16 & 0x7fffu) > 0x7f80u) ? (unsigned)(2047 - (tid * 4 + e)) : ((ord << 16) | (unsigned)(2047 - (tid * 4 + e)));
                m2 = max(m2, min(m1, key)); m1 = max(m1, key); }
            TOP2_STEP(__builtin_amdgcn_update_dpp(0u, m1, 0xB1, 0xf, 0xf, false), __builtin_amdgcn_update_dpp(0u, m2, 0xB1, 0xf, 0xf, false));
            TOP2_STEP(__builtin_amdgcn_update_dpp(0u, m1, 0x4E, 0xf, 0xf, false), __builtin_amdgcn_update_dpp(0u, m2, 0x4E, 0xf, 0xf, false));
            TOP2_STEP(__builtin_amdgcn_update_dpp(0u, m1, 0x141, 0xf, 0xf, false), __builtin_amdgcn_update_dpp(0u, m2, 0x141, 0xf, 0xf, false));
            TOP2_STEP(__builtin_amdgcn_update_dpp(0u, m1, 0x140, 0xf, 0xf, false), __builtin_amdgcn_update_dpp(0u, m2, 0x140, 0xf, 0xf, false));
            { auto a1 = __builtin_amdgcn_permlane16_swap(m1, m1, false, false); auto a2 = __builtin_amdgcn_permlane16_swap(m2, m2, false, false);
              const unsigned lo1 = a1[0], hi1 = a1[1], s2 = max(a2[0], a2[1]); m2 = max(min(lo1, hi1), s2); m1 = max(lo1, hi1); }
            { auto a1 = __builtin_amdgcn_permlane32_swap(m1, m1, false, false); auto a2 = __builtin_amdgcn_permlane32_swap(m2, m2, false, false);
              const unsigned lo1 = a1[0], hi1 = a1[1], s2 = max(a2[0], a2[1]); m2 = max(min(lo1, hi1), s2); m1 = max(lo1, hi1); }
            if (lane == 0) { sk[2 * wid] = m1; sk[2 * wid + 1] = m2; }
        }
        STAMPP(c, 102 + t);
        if (t == 9) WAITV(9); else if (t == 10) WAITV(9); else if (t <= 12) WAITV(7); else if (t == 13) WAITV(6); else if (t == 14) WAITV(2); else WAITV(0);
        BARRIER(); STAMPP(c, 70 + t); ROT();
        if (t == 15 && tid == 0) set_flag(kargs()->fl, FL_KV + h * 8 + x, E);
    }
    P2_GRP(1, 1, 0); P2_GRP(1, 2, 1); P2_GRP(1, 3, 2);
    bf16x8 xa[2];
#pragma unroll
    for (int ks = 0; ks < 2; ++ks) xa[ks] = bld16(c.ra2, lc < 2 ? (unsigned)(lc ? ti2 : ti1) * (M * 2) + (xcd * 64 + ks * 32 + lg * 8) * 2u : 0x7ffffff0u, 0u);
    STAMP(c, 3); STAMPRT(c, 119);
    BARRIER();
    {
        const lds_p qw = (lds_p)lds_raw + (wm2 < 2 ? 0 : 131072) + (wm2 & 1) * 16384 + wn2 * 2048 + lane * 16;
#pragma unroll
        for (int mt = 0; mt < 4; ++mt)
#pragma unroll
            for (int j = 0; j < 2; ++j) *(__attribute__((address_space(3))) bf16x8*)(qw + (mt >> 1) * 8192 + (mt & 1) * 4096 + j * 1024) = pack2(acq[2 * j][mt] * QSCALE, acq[2 * j + 1][mt] * QSCALE);
    }
    BARRIER();
    const kargs_t P2 = kargs();
    unsigned* const fl = P2->fl;
    AttnCtx a;
    a.lane = lane;
    {
        const lds_p qr = (lds_p)lds_raw + (wid < 4 ? 0 : 131072) + (wid & 3) * 8192 + lane * 16;
#pragma unroll
        for (int i = 0; i < 8; ++i) a.qf[i >> 2][i & 3] = *(lfrag_p)(qr + i * 1024);
    }
    STAMP(a, 4);
    if (ffw) {
        const lds_p tb = (lds_p)lds_raw + 114688 + (wid >> 2) * 8192 + ffn_tr_base(lane, wid & 3);
        f32x4 d = {};
#pragma unroll
        for (int ks = 0; ks < 2; ++ks) d = MFMA16(xa[ks], cat8(trd(tb + ks * 4096), trd(tb + ks * 4096 + 512)), d);
        if (lane < 16) {
            float* pp = P2->p1 + (((jj * 8 + xcd) * 2 + (wid >> 2)) * 2) * 64 + (wid & 3) * 16 + lane;
            __hip_atomic_store((gu32*)pp, __float_as_uint(d[0]), __ATOMIC_RELAXED, __HIP_MEMORY_SCOPE_AGENT); __hip_atomic_store((gu32*)(pp + 64), __float_as_uint(d[1]), __ATOMIC_RELAXED, __HIP_MEMORY_SCOPE_AGENT);
        }
    }
    if (wid == 0) wait_flags(fl, FL_KV + h * 8, 1, 8, E, lane);
    BARRIER();
    STAMP(a, 5); STAMPRT(a, 120);
    const int grp = wid >> 2; a.wq = wid & 3;
    a.h = h;
    a.blkP = 15 - x; a.blkR = x;
    a.usw = grp ? 1000 : 4 * x + 4;
    a.delta = 30 - 4 * x;
    a.rowblk = (grp ? a.blkP : a.blkR) * 4 + a.wq;
    a.rc = mk_rsrc(P2->ctx, S * HD * 2); a.rk = mk_rsrc(P2->Kf, S * HD * 2); a.rv = mk_rsrc(P2->Vf, S * HD * 2);
    const unsigned ldsb = (unsigned)(uintptr_t)(lds_p)lds_raw + grp * 65536;
    const unsigned kdst = ldsb + a.wq * 2048, vdst = kdst + 32768;
    a.voff = a.lane * 16;
    const unsigned o0 = (unsigned)(a.h * 64) * 8192u + a.wq * 2048u;
    SET_M0(kdst, 0); SBAR(); DMA_BC(a.rk, a.voff, o0, 0); DMA_BC(a.rk, a.voff, o0, 1024);
    SET_M0(kdst, 8192); SBAR(); DMA_BC(a.rk, a.voff, o0 + 8192, 0); DMA_BC(a.rk, a.voff, o0 + 8192, 1024);
    SET_M0(vdst, 24576); SBAR(); DMA_BC(a.rv, a.voff, o0, 0); DMA_BC(a.rv, a.voff, o0, 1024);
    SET_M0(vdst, 0); SBAR(); DMA_BC(a.rv, a.voff, o0, 0); DMA_BC(a.rv, a.voff, o0, 1024);
    SET_M0(kdst, 16384); SBAR(); DMA_BC(a.rk, a.voff, o0 + 16384, 0); DMA_BC(a.rk, a.voff, o0 + 16384, 1024);
    SET_M0(vdst, 8192); SBAR(); DMA_BC(a.rv, a.voff, o0 + 8192, 0); DMA_BC(a.rv, a.voff, o0 + 8192, 1024);
    if (grp) { SET_M0(vdst, 16384); SBAR(); DMA_BC(a.rv, a.voff, o0 + 16384, 0); DMA_BC(a.rv, a.voff, o0 + 16384, 1024); }
    else     { SET_M0(kdst, 24576); SBAR(); DMA_BC(a.rk, a.voff, o0 + 24576, 0); DMA_BC(a.rk, a.voff, o0 + 24576, 1024); }
    {
        unsigned long long m;
        if (grp) m = ~0ull << a.rowblk;
        else {
            m = (~0ull << a.rowblk) & ~(~0ull << a.usw);
            const int uP = a.blkP * 4 + a.wq - a.delta;
            m |= ~0ull << (uP > a.usw ? uP : a.usw);
        }
        a.spmask = m;
    }
    a.qlds = (unsigned)(uintptr_t)(lds_p)lds_raw + 131072 + a.wq * 8192 + lane * 16;
    a.kdst = kdst; a.vdst = vdst;
    a.f1flag = ffw ? fl + 32 * (FL_F1 + jj * 8 + xcd) : nullptr; a.E = E;
    a.go1 = o0 + (grp ? 3u : 2u) * 8192u; a.go2 = o0 + (grp ? 3u : 4u) * 8192u;
    a.fbase = (lds_p)lds_raw + grp * 65536 + a.lane * 16;
#pragma unroll
    for (int db = 0; db < 8; ++db) { a.o[db][0] = f32x4{}; a.o[db][1] = f32x4{}; }
    a.negm[0] = a.negm[1] = a.l4[0] = a.l4[1] = f32x4{}; a.mref[0] = a.mref[1] = 0.f; a.bad = 0ull;
    a.pA[0] = a.pA[1] = a.pB[0] = a.pB[1] = u32x4{};
#pragma unroll
    for (int i = 0; i < 8; ++i) a.ones[i] = (short)0x3f80;
    PIN(a.ones); PIN(a.negm[0]); PIN(a.negm[1]);
    WAITV(12);
    STAMP(a, 11);
    BARRIER();
    STAMP(a, 12);
    if (grp) attn_stream<true>(a); else attn_stream<false>(a);
    WAITV(0);
    if (grp) BARRIER();
    BARRIER();
    STAMP(a, 98); STAMPRT(a, 121);
    unsigned p1[8];
    const kargs_t P3 = kargs();
    if (ffw) {
        { const int krow = wid * 8 + (lane >> 3); const unsigned vo = (unsigned)(jj * 64 + krow) * (M * 2) + xcd * 128u + ((((((lane & 7) >> 1) ^ ffn_swz(krow)) << 1) | (lane & 1)) * 16u);
          SET_M0((unsigned)(uintptr_t)(lds_p)lds_raw + 131072u + wid * 1024u, 0); SBAR(); DMA_B(mk_rsrc(P3->wd, F * M * 2), vo, 0u, 0); }
        if (wid == 0) wait_flags(P3->fl, FL_F1 + jj * 8, 1, 8, E, lane);
        BARRIER();
        if (tid < 256) {
#pragma unroll
            for (int kg = 0; kg < 8; ++kg) p1[kg] = ld_agent(P3->p1 + (jj * 8 + kg) * 256 + tid);
        }
    }
    {
        const int wq = a.wq, lane = a.lane;
        const lds_p xw = (lds_p)lds_raw + (grp * 4 + wq) * 8192 + lane * 16;
        const lds_p xr = (lds_p)lds_raw + ((grp ^ 1) * 4 + wq) * 8192 + lane * 16;
        const lds_p mw = (lds_p)lds_raw + 65536 + (grp * 4 + wq) * 1024 + lane * 16, mr = (lds_p)lds_raw + 65536 + ((grp ^ 1) * 4 + wq) * 1024 + lane * 16;
        a.bad |= __ballot(!(a.l4[0][0] <= LSUM_LIMIT && a.l4[1][0] <= LSUM_LIMIT));
        if (grp) {
#pragma unroll
            for (int db = 0; db < 4; ++db)
#pragma unroll
                for (int qb = 0; qb < 2; ++qb) { const f32x4 t = a.o[db][qb]; a.o[db][qb] = a.o[4 + db][qb]; a.o[4 + db][qb] = t; }
        }
#pragma unroll
        for (int i = 0; i < 8; ++i) *(lf4_p)(xw + i * 1024) = a.o[4 + (i >> 1)][i & 1];
        *(lf4_p)mw = (f32x4){a.mref[0], a.mref[1], a.l4[0][0], a.l4[1][0]};
        if (lane == 0) *(__attribute__((address_space(3))) unsigned*)((lds_p)lds_raw + 65536 + 8192 + wid * 4) = a.bad ? 1u : 0u;
        BARRIER();
        STAMP(a, 99);
        const f32x4 ml = *(lf4_p)mr;
        float wo_[2], wp[2];
#pragma unroll
        for (int qb = 0; qb < 2; ++qb) {
            const float mm = __builtin_fmaxf(a.mref[qb], ml[qb]), fo = __builtin_amdgcn_exp2f(a.mref[qb] - mm), fp = __builtin_amdgcn_exp2f(ml[qb] - mm);
            const float il = __builtin_amdgcn_rcpf(a.l4[qb][0] * fo + ml[2 + qb] * fp); wo_[qb] = fo * il; wp[qb] = fp * il; }
#pragma unroll
        for (int i = 0; i < 8; ++i) { const f32x4 p = *(lf4_p)(xr + i * 1024); a.o[i >> 1][i & 1] = a.o[i >> 1][i & 1] * wo_[i & 1] + p * wp[i & 1]; }
        const float one[2] = {1.f, 1.f};
        attn_store(a.rc, a.o, one, a.voff + ((unsigned)(a.blkP * 8 + wq * 2) * 128u + a.h * 4 + grp * 2) * 1024u, 0, 2);
        const u32x4 b0 = *(__attribute__((address_space(3))) u32x4*)((lds_p)lds_raw + 65536 + 8192), b1 = *(__attribute__((address_space(3))) u32x4*)((lds_p)lds_raw + 65536 + 8192 + 16);
        if (UNLIKELY(__builtin_amdgcn_readfirstlane(b0[0] | b0[1] | b0[2] | b0[3] | b1[0] | b1[1] | b1[2] | b1[3]))) {
            WAITV(0); BARRIER();
            attn_exact(P3->x, P3->wq, a.rk, a.rv, a.rc, a.h, (grp ? a.blkP : a.blkR) * 4 + wq, lane);
        }
    }
    WAITV(0);
    BARRIER();
    if (tid == 0) { set_flag(P3->fl, FL_CTX + x * 32 + h, E); set_flag(P3->fl, FL_CTX + (15 - x) * 32 + h, E); }
    auto ffn2 = [&]() {
        if (ffw) {
            float* red = (float*)(lds_raw + 143360); u16* hb = (u16*)(lds_raw + 143360 + 2048);
            if (tid < 256) {
                float t = 0.f;
#pragma unroll
                for (int kg = 0; kg < 8; ++kg) t += __uint_as_float(p1[kg]);
                red[tid] = rbf(t);
            }
            WAITV(0);
            BARRIER();
            if (tid < 128) { const float g = red[tid], u = red[128 + tid]; hb[tid] = (u16)(cvtpk(rbf(g / (1.f + __expf(-g))) * u, 0.f) & 0xffffu); }
            BARRIER();
            if (wid < 4) {
                const lds_p tb = (lds_p)lds_raw + 131072 + ffn_tr_base(lane, wid);
                f32x4 d = {};
#pragma unroll
                for (int ks = 0; ks < 2; ++ks) { bf16x8 ha = {}; if (lc < 2) ha = *(const bf16x8*)(hb + lc * 64 + ks * 32 + lg * 8);
                    d = MFMA16(ha, cat8(trd(tb + ks * 4096), trd(tb + ks * 4096 + 512)), d); }
                if (lane < 16) { float* pp = P3->p2 + (jj * 2) * M + xcd * 64 + wid * 16 + lane;
                    __hip_atomic_store((gu32*)pp, __float_as_uint(d[0]), __ATOMIC_RELAXED, __HIP_MEMORY_SCOPE_AGENT); __hip_atomic_store((gu32*)(pp + M), __float_as_uint(d[1]), __ATOMIC_RELAXED, __HIP_MEMORY_SCOPE_AGENT); }
            }
        }
    };
    auto ffn2_flag = [&]() { if (ffw && tid == 0) set_flag(P3->fl, FL_F2 + xcd * 28 + jj, E); };
    STAMP(a, 60); STAMPRT(a, 122);
    out_phase_sk(a, lds_raw, kargs(), ti1, ti2, rsel0, rsel1, E, ffn2, ffn2_flag);
    if (bid == 0) { WAITV(0); BARRIER(); if (tid == 0) set_flag(kargs()->fl, 0, E); }
    STAMP_END(a, kargs()->dbg);
}

extern "C" void kernel_launch(void* const* d_in, const int* in_sizes, int n_in, void* d_out, int out_size, void* d_ws, size_t ws_size, hipStream_t stream) {
    char* ws = (char*)d_ws;
    Params P;
    P.x = (const u16*)d_in[0]; P.ckv = (const u16*)d_in[1]; P.wq = (const u16*)d_in[2]; P.wk = (const u16*)d_in[3]; P.wv = (const u16*)d_in[4]; P.wo = (const u16*)d_in[5];
    P.wr = (const u16*)d_in[7]; P.wg = (const u16*)d_in[8]; P.wu = (const u16*)d_in[9]; P.wd = (const u16*)d_in[10]; P.gw = (const u16*)d_in[11];
    P.Kf = (u16*)(ws + WS_K); P.Vf = (u16*)(ws + WS_V); P.ctx = (u16*)(ws + WS_CTX); P.out = (u16*)d_out;
    P.r = (unsigned long long*)(ws + WS_R); P.g = (unsigned long long*)(ws + WS_G); P.p1 = (float*)(ws + WS_HB); P.p2 = (float*)(ws + WS_P2); P.slab = (float*)(ws + WS_SLAB);
    P.fl = (unsigned*)(ws + WS_FLAG); P.dbg = (unsigned long long*)(ws + (128u << 20));
    (void)hipFuncSetAttribute((const void*)fused_kernel, hipFuncAttributeMaxDynamicSharedMemorySize, PROJ_LDS);
    fused_kernel<<<256, 512, PROJ_LDS, stream>>>(P);
}
```
